```python
import torch

def _softmax(x, axis=-1):
    return torch.softmax(x, dim=axis)
def _gelu(x):
    return torch.nn.functional.gelu(x, approximate="tanh")
def _silu(x):
    return torch.nn.functional.silu(x)
def _sigmoid(x):
    return torch.sigmoid(x)
def _layernorm(x, axis=-1, eps=1e-5):
    m = x.mean(dim=axis, keepdim=True)
    v = x.var(dim=axis, keepdim=True, unbiased=False)
    return (x - m) / torch.sqrt(v + eps)
def _scatter_add(tgt, idx, src, axis=0):
    return tgt.clone().index_add_(axis, idx, src)
def _segment_sum(src, seg, n):
    out = torch.zeros((n,) + src.shape[1:], dtype=src.dtype)
    return out.index_add_(0, seg, src)
def _pad(x, pw):
    flat = tuple(v for pair in reversed(pw) for v in pair)
    return torch.nn.functional.pad(x, flat)

kernel_name = 'parallel_sum__sliding_block__dense_activation_gelu__prompt'

def setup_inputs():

    _w = (lambda a: (lambda t: t.to(torch.bfloat16) if t.is_floating_point() else t)(torch.as_tensor(a)))
    def _A_setup():
        B, S, D = 1, 2048, 576
        return {
            "x": _w((lambda *s: torch.randn(*s, dtype=torch.bfloat16) * 0.05)(B, S, D)),
            "wqkv": _w((lambda *s: torch.randn(*s, dtype=torch.bfloat16) * 0.05)(D, 3 * D)),
            "wo": _w((lambda *s: torch.randn(*s, dtype=torch.bfloat16) * 0.05)(D, D)),
        }
    _A_in = _A_setup()

    def _B_setup():
        B, S, D, F = 1, 2048, 576, 4608
        return {
            "x": _w((lambda *s: torch.randn(*s, dtype=torch.bfloat16) * 0.05)(B, S, D)),
            "w1": _w((lambda *s: torch.randn(*s, dtype=torch.bfloat16) * 0.8)(D, F) * (D ** -0.5)),
            "b1": _w((lambda *s: torch.randn(*s, dtype=torch.bfloat16) * 0.8)(F) * 0.01),
            "w2": _w((lambda *s: torch.randn(*s, dtype=torch.bfloat16) * 0.8)(F, D) * (F ** -0.5)),
            "b2": _w((lambda *s: torch.randn(*s, dtype=torch.bfloat16) * 0.8)(D) * 0.01),
        }
    _B_in = _B_setup()

    return {
        "A_x": _A_in["x"],
        "A_wqkv": _A_in["wqkv"],
        "A_wo": _A_in["wo"],
        "B_x": _B_in["x"],
        "B_w1": _B_in["w1"],
        "B_b1": _B_in["b1"],
        "B_w2": _B_in["w2"],
        "B_b2": _B_in["b2"],
    }

def reference(A_x, A_wqkv, A_wo, B_x, B_w1, B_b1, B_w2, B_b2):
    def _A_ref(x, wqkv, wo):
        B, S, D = x.shape
        H = 32; Dh = D // H; W = 256
        qkv = (lambda x, *d: x.permute(*d))((x @ wqkv).reshape(B, S, 3, H, Dh), 2, 0, 3, 1, 4)
        q, k, v = qkv[0], qkv[1], qkv[2]
        scores = torch.einsum("bhqd,bhkd->bhqk", q, k) * (Dh ** -0.5)
        idx = torch.arange(S)
        mask = (idx[:, None] - idx[None, :] >= 0) & (idx[:, None] - idx[None, :] < W)
        scores = torch.where(mask[None, None], scores, -1e9)
        p = _softmax(scores, axis=-1)
        o = torch.einsum("bhqk,bhkd->bhqd", p, v)
        return (lambda x, *d: x.permute(*d))(o, 0, 2, 1, 3).reshape(B, S, D) @ wo

    def _B_ref(x, w1, b1, w2, b2):
        h = torch.einsum("bsd,df->bsf", x, w1) + b1
        h = _gelu(h)
        return torch.einsum("bsf,fd->bsd", h, w2) + b2

    x = A_x
    xn = _layernorm(x, axis=-1)
    return x + _A_ref(xn, A_wqkv, A_wo) + _B_ref(xn, B_w1, B_b1, B_w2, B_b2)
```

```cpp
#include <hip/hip_runtime.h>
#include <hip/hip_bf16.h>
typedef unsigned short u16;
__device__ __forceinline__ float bf2f(u16 v) { return __uint_as_float((unsigned)v << 16); }
__device__ __forceinline__ u16 f2bf(float f) { __hip_bfloat16 b = __float2bfloat16(f); return *(u16*)&b; }
__device__ __forceinline__ float rbf(float f) { return bf2f(f2bf(f)); }

__global__ void ln_k(const u16* x, u16* xn) {
  int row = blockIdx.x, t = threadIdx.x;
  float v[9], s = 0;
  for (int i = 0; i < 9; ++i) { v[i] = bf2f(x[row * 576 + t + 64 * i]); s += v[i]; }
  for (int o = 32; o; o >>= 1) s += __shfl_xor(s, o);
  float mean = s / 576.f, q = 0;
  for (int i = 0; i < 9; ++i) q += (v[i] - mean) * (v[i] - mean);
  for (int o = 32; o; o >>= 1) q += __shfl_xor(q, o);
  float var = rbf(q / 576.f), mb = rbf(mean);
  float sd = rbf(sqrtf(rbf(var + 1e-5f)));
  float rs = 1.f / sd;
  for (int i = 0; i < 9; ++i) xn[row * 576 + t + 64 * i] = f2bf((v[i] - mb) * rs);
}
template<int MODE>
__global__ void gemm_k(const u16* A, const u16* B, const u16* bias, u16* C, int N, int K) {
  int n = blockIdx.x * 64 + threadIdx.x, m0 = blockIdx.y * 8;
  float acc[8] = {0};
  for (int k = 0; k < K; ++k) {
    float b = bf2f(B[(size_t)k * N + n]);
    for (int i = 0; i < 8; ++i) acc[i] += bf2f(A[(size_t)(m0 + i) * K + k]) * b;
  }
  for (int i = 0; i < 8; ++i) {
    float v = acc[i];
    if (MODE == 1) { v += bf2f(bias[n]); float u = 0.7978845608f * (v + 0.044715f * v * v * v); v = v / (1.f + __expf(-2.f * u)); }
    C[(size_t)(m0 + i) * N + n] = f2bf(v);
  }
}
__global__ void attn_k(const u16* qkv, u16* o) {
  int q = blockIdx.x * 64 + threadIdx.x, h = blockIdx.y;
  float qv[18], acc[18] = {0}, mx = -1e30f, l = 0;
  for (int d = 0; d < 18; ++d) qv[d] = bf2f(qkv[q * 1728 + h * 18 + d]);
  int k0 = q - 255 < 0 ? 0 : q - 255;
  for (int k = k0; k <= q; ++k) { float s = 0; for (int d = 0; d < 18; ++d) s += qv[d] * bf2f(qkv[k * 1728 + 576 + h * 18 + d]); mx = fmaxf(mx, s); }
  for (int k = k0; k <= q; ++k) {
    float s = 0; for (int d = 0; d < 18; ++d) s += qv[d] * bf2f(qkv[k * 1728 + 576 + h * 18 + d]);
    float p = rbf(__expf((s - mx) * 0.23570226f)); l += p;
    for (int d = 0; d < 18; ++d) acc[d] += p * bf2f(qkv[k * 1728 + 1152 + h * 18 + d]);
  }
  for (int d = 0; d < 18; ++d) o[q * 576 + h * 18 + d] = f2bf(acc[d] / l);
}
__global__ void final_k(const u16* x, const u16* o, const u16* wo, const u16* h, const u16* w2, const u16* b2, u16* out) {
  int n = blockIdx.x * 64 + threadIdx.x, m0 = blockIdx.y * 8;
  float acc[8] = {0};
  for (int k = 0; k < 576; ++k) { float b = bf2f(wo[k * 576 + n]); for (int i = 0; i < 8; ++i) acc[i] += bf2f(o[(m0 + i) * 576 + k]) * b; }
  for (int k = 0; k < 4608; ++k) { float b = bf2f(w2[k * 576 + n]); for (int i = 0; i < 8; ++i) acc[i] += bf2f(h[(size_t)(m0 + i) * 4608 + k]) * b; }
  for (int i = 0; i < 8; ++i) out[(m0 + i) * 576 + n] = f2bf(acc[i] + bf2f(b2[n]) + bf2f(x[(m0 + i) * 576 + n]));
}
extern "C" void kernel_launch(void* const* d_in, const int* in_sizes, int n_in, void* d_out, int out_size, void* d_ws, size_t ws_size, hipStream_t stream) {
  const u16* x = (const u16*)d_in[0]; const u16* wqkv = (const u16*)d_in[1]; const u16* wo = (const u16*)d_in[2];
  const u16* w1 = (const u16*)d_in[4]; const u16* b1 = (const u16*)d_in[5]; const u16* w2 = (const u16*)d_in[6]; const u16* b2 = (const u16*)d_in[7];
  u16* xn = (u16*)d_ws; u16* qkv = xn + 2048 * 576; u16* h = qkv + 2048 * 1728; u16* o = h + 2048 * 4608;
  ln_k<<<2048, 64, 0, stream>>>(x, xn);
  gemm_k<0><<<dim3(27, 256), 64, 0, stream>>>(xn, wqkv, nullptr, qkv, 1728, 576);
  gemm_k<1><<<dim3(72, 256), 64, 0, stream>>>(xn, w1, b1, h, 4608, 576);
  attn_k<<<dim3(32, 32), 64, 0, stream>>>(qkv, o);
  final_k<<<dim3(9, 256), 64, 0, stream>>>(x, o, wo, h, w2, b2, (u16*)d_out);
}
```

```cpp
#include <hip/hip_runtime.h>
#pragma clang diagnostic ignored "-Wint-to-pointer-cast"
#if defined(__HIP_DEVICE_COMPILE__)
#pragma clang attribute push (__attribute__((target("no-packed-fp32-ops"))), apply_to = function)
#endif
typedef unsigned u32;
typedef unsigned long long u64;
typedef short s16x4 __attribute__((ext_vector_type(4)));
typedef __bf16 bf16x8 __attribute__((ext_vector_type(8)));
typedef __bf16 bf16x2 __attribute__((ext_vector_type(2)));
typedef float f32x2 __attribute__((ext_vector_type(2)));
typedef float f32x4 __attribute__((ext_vector_type(4)));
typedef float f32x16 __attribute__((ext_vector_type(16)));
typedef u32 u32x2 __attribute__((ext_vector_type(2)));
typedef u32 u32x4 __attribute__((ext_vector_type(4)));
#define LAS __attribute__((address_space(3)))
#define GAS __attribute__((address_space(1)))
#define DEV __device__ __forceinline__
#define RLX_AGENT __ATOMIC_RELAXED, __HIP_MEMORY_SCOPE_AGENT

constexpr u32 WS_EPOCH = 0;
constexpr u32 WS_FLAG = 1024;
constexpr u32 QKV_SZ = 3 * 36 * 1024;
constexpr u32 WS_QKV = 131072;
constexpr u32 G_SZ = 41 * 8192;
constexpr u32 WS_G = WS_QKV + 64 * QKV_SZ;
constexpr u32 WS_P = WS_G + 64 * G_SZ;
constexpr u32 WS_END = WS_P + 256 * 27648;
constexpr u32 OOB = 0x7ffffff0u;
constexpr u32 PP = 3104;
constexpr u32 L_KV = 98304, L_BIAS = 157312, L_RS = 159104, L_MSK = 159616, L_DUMP = 161664, L_TOTAL = 163840;
constexpr u32 SLOT_A = 24576, SLOT_B = 18432;
constexpr u32 SENT = 0xffffffffu;
constexpr int NT = 2;

DEV u32x4 make_srd(const void* p, u32 bytes) { u64 a = (u64)p; return u32x4{(u32)a, (u32)(a >> 32) & 0xffffu, bytes, 0x00020000u}; }
template <int LI, int SI> DEV void dma16(u32x4 srd, u32 voff, u32 soff, u32 lds) {
  u32 t;
  asm volatile("s_add_u32 m0, %1, %5\n\ts_add_u32 %0, %4, %6\n\tbuffer_load_dwordx4 %2, %3, %0 offen lds"
               : "=&s"(t) : "s"(lds), "v"(voff), "s"(srd), "s"(soff), "i"(LI), "i"(SI) : "memory", "scc");
}
template <int LI, int SI> DEV void dma16c(u32x4 srd, u32 voff, u32 soff, u32 lds) {
  u32 t;
  asm volatile("s_add_u32 m0, %1, %5\n\ts_add_u32 %0, %4, %6\n\tbuffer_load_dwordx4 %2, %3, %0 offen sc1 lds"
               : "=&s"(t) : "s"(lds), "v"(voff), "s"(srd), "s"(soff), "i"(LI), "i"(SI) : "memory", "scc");
}
template <int LI> DEV void dma16s(u32x4 srd, u32 voff, u32& so, u32 inc, u32 lds) {
  asm volatile("s_add_u32 m0, %1, %5\n\ts_add_u32 %0, %0, %4\n\tbuffer_load_dwordx4 %2, %3, %0 offen lds"
               : "+s"(so) : "s"(lds), "v"(voff), "s"(srd), "s"(inc), "i"(LI) : "memory", "scc");
}
DEV void touch(u32x4 srd, u32 voff, u32 lds) {
  asm volatile("s_mov_b32 m0, %0\n\ts_nop 0\n\tbuffer_load_dword %1, %2, 0 offen lds" :: "s"(lds), "v"(voff), "s"(srd) : "memory");
}
template <int LI> DEV void dma16m(u32x4 srd, u32 voff, u32 soff, u32 lds, u64 mask) {
  asm volatile("s_add_u32 m0, %0, %5\n\ts_mov_b64 exec, %4\n\tbuffer_load_dwordx4 %1, %2, %3 offen lds\n\ts_mov_b64 exec, -1"
               :: "s"(lds), "v"(voff), "s"(srd), "s"(soff), "s"(mask), "i"(LI) : "memory", "scc");
}
template <int OFF> DEV u32x4 ld16(u32x4 srd, u32 voff, u32 soff) {
  u32x4 r; asm volatile("buffer_load_dwordx4 %0, %1, %2, %3 offen offset:%4" : "=v"(r) : "v"(voff), "s"(srd), "s"(soff), "i"(OFF) : "memory"); return r;
}
template <int OFF> DEV void st16c(u32x4 v, u32x4 srd, u32 voff, u32 soff) {
  asm volatile("buffer_store_dwordx4 %0, %1, %2, %3 offen offset:%4 sc1\n\ts_nop 1" :: "v"(v), "v"(voff), "s"(srd), "s"(soff), "i"(OFF) : "memory");
}
template <int OFF> DEV void st8c(u32x2 v, u32x4 srd, u32 voff, u32 soff) {
  asm volatile("buffer_store_dwordx2 %0, %1, %2, %3 offen offset:%4 sc1\n\ts_nop 1" :: "v"(v), "v"(voff), "s"(srd), "s"(soff), "i"(OFF) : "memory");
}
DEV void st4c(u32 v, u32x4 srd, u32 voff, u32 soff) {
  asm volatile("buffer_store_dword %0, %1, %2, %3 offen sc1\n\ts_nop 1" :: "v"(v), "v"(voff), "s"(srd), "s"(soff) : "memory");
}
template <int OFF> DEV void st8(u32x2 v, u32x4 srd, u32 voff, u32 soff) {
  asm volatile("buffer_store_dwordx2 %0, %1, %2, %3 offen offset:%4\n\ts_nop 1" :: "v"(v), "v"(voff), "s"(srd), "s"(soff), "i"(OFF) : "memory");
}
template <class T> DEV void pin(T& v) { asm volatile("" : "+v"(v)); }
#define STAMP(i)
#define STAMPR(i)
template <int N> DEV void wait_vm() { asm volatile("s_waitcnt vmcnt(%0)" :: "i"(N) : "memory"); }
DEV void wait_lgkm0() { asm volatile("s_waitcnt lgkmcnt(0)" ::: "memory"); }
DEV void fence_sched() { __builtin_amdgcn_sched_barrier(0); }
DEV void bar() { fence_sched(); __builtin_amdgcn_s_barrier(); fence_sched(); }

DEV float u2f(u32 v) { return __builtin_bit_cast(float, v); }
DEV u32 f2u(float v) { return __builtin_bit_cast(u32, v); }
DEV bf16x8 as_bf(u32x4 v) { return __builtin_bit_cast(bf16x8, v); }
DEV u32 pk2(float a, float b) { return __builtin_bit_cast(u32, __builtin_convertvector(f32x2{a, b}, bf16x2)); }
DEV float blo(u32 d) { return u2f(d << 16); }
DEV float bhi(u32 d) { return u2f(d & 0xffff0000u); }
DEV float rbf(float f) { return blo(pk2(f, f)); }
DEV float max3(float a, float b, float c) { return __builtin_elementwise_maximum(__builtin_elementwise_maximum(a, b), c); }
DEV u32 umax(u32 a, u32 b) { return a > b ? a : b; }
DEV u32 umin(u32 a, u32 b) { return a < b ? a : b; }
DEV bool all_lanes(bool p) { return __builtin_amdgcn_ballot_w64(!p) == 0; }
DEV float dot2(u32 a, u32 b, float c) { return __builtin_amdgcn_fdot2_f32_bf16(__builtin_bit_cast(bf16x2, a), __builtin_bit_cast(bf16x2, b), c, false); }
DEV float quad_sum(float v) {
  v += u2f(__builtin_amdgcn_mov_dpp(f2u(v), 0xb1, 0xf, 0xf, true));
  return v + u2f(__builtin_amdgcn_mov_dpp(f2u(v), 0x4e, 0xf, 0xf, true));
}
DEV u32x2 lds_r8(u32 a) { return *(LAS u32x2*)a; }
DEV u32x4 cat(u32x2 a, u32x2 b) { return u32x4{a[0], a[1], b[0], b[1]}; }
DEV u32x4 lds_r16(u32 a) { return *(LAS u32x4*)a; }
DEV void lds_w16(u32 a, u32x4 v) { *(LAS u32x4*)a = v; }
DEV bf16x8 tr_frag(u32 a0, u32 a1) {
  s16x4 lo = __builtin_amdgcn_ds_read_tr16_b64_v4i16((LAS s16x4*)a0);
  s16x4 hi = __builtin_amdgcn_ds_read_tr16_b64_v4i16((LAS s16x4*)a1);
  return __builtin_bit_cast(bf16x8, __builtin_shufflevector(lo, hi, 0, 1, 2, 3, 4, 5, 6, 7));
}
#define MFMA32(a, b, c) __builtin_amdgcn_mfma_f32_32x32x16_bf16((a), (b), (c), 0, 0, 0)
#define MFMA16(a, b, c) __builtin_amdgcn_mfma_f32_16x16x32_bf16((a), (b), (c), 0, 0, 0)

constexpr float GK1 = -0.10294324f, GK2 = -2.3022082f;
DEV float gelu_a(float x, float k1, float k2, float zb) { return __builtin_amdgcn_exp2f(__builtin_fmaf(x, __builtin_fmaf(x * x, k1, k2), zb)); }
DEV float gelu_b(float x, float e) { return x * __builtin_amdgcn_rcpf(1.f + e); }
DEV void poll_words(const char* ws, u32 off, bool active, u64 tag) {
#pragma unroll 1
  for (u32 spins = 0; spins < (1u << 18); ++spins) {
    u64 v = active ? __hip_atomic_load((GAS u64*)(ws + off), RLX_AGENT) : tag;
    if (all_lanes(v == tag)) break;
    __builtin_amdgcn_s_sleep(2);
  }
  __builtin_amdgcn_fence(__ATOMIC_ACQUIRE, "wavefront");
}
DEV int bid_of(int m, int kq, int s) { return ((m & 7) * 4 + s) * 8 + (m >> 3) * 4 + kq; }
constexpr u64 diag_mask(int e) {
  u64 mk = 0;
  for (int l = 0; l < 64; ++l) if ((e & 3) + 8 * (e >> 2) + 4 * (l >> 5) <= (l & 31)) mk |= 1ull << l;
  return mk;
}

__global__ __launch_bounds__(512, 2) void fused_block(const u32* __restrict__ x, const u32* __restrict__ wqkv, const u32* __restrict__ wo,
                                                      const u32* __restrict__ w1, const u32* __restrict__ b1, const u32* __restrict__ w2,
                                                      const u32* __restrict__ b2, u32* __restrict__ out, char* __restrict__ ws) {
  __shared__ __attribute__((aligned(1024))) char smem[L_TOTAL];
  const u32 LB = (u32)(size_t)(LAS char*)smem;
  const int tid = __builtin_amdgcn_workitem_id_x(), l = tid & 63;
  const int w = __builtin_amdgcn_readfirstlane(tid >> 6) & 7, r = w & 3, c = w >> 2;
  const int bid = __builtin_amdgcn_workgroup_id_x(), kq = bid & 3, mh = (bid >> 2) & 1, m = 8 * mh + (bid >> 5), s = (bid >> 3) & 3;
  const u32x4 sWS = make_srd(ws, WS_END);
  STAMPR(62); STAMP(0);
  const int h = l >> 5, t16 = l & 15, g4 = l >> 4, q4 = (l & 15) >> 2, p4 = l & 3, blk = (l >> 4) & 1;
  const u32 xso = (128 * m + 32 * r + 16 * c) * 1152;
  const bool isq = s < 3;
  const int NC = isq ? 7 : 6;
  const int ql0 = 2 * kq + (kq < 2 ? 2 : 0), ql1 = 2 * kq + (kq < 2 ? 0 : 1);
  const u32x4 sQKV = make_srd(wqkv, 576 * 3456), sW1 = make_srd(w1, 576 * 9216);
  const u32 pcs = 16 * ((l & 7) ^ (4 * blk));
  const u32 vo_q = (l >> 3) * 3456 + pcs, vo_f = (l >> 3) * 9216 + pcs;
  const u32 dl = LB + w * 3072;
  const u32 fcol = 2304 * kq + 512 * s;
  u32x4 sA = isq ? sQKV : sW1;
  u32 voA = isq ? (kq < 2 ? vo_q : vo_q ^ 64) : vo_f, i8 = isq ? 8 * 3456 : 8 * 9216, i176 = 22 * i8;
  u32 so = 3 * w * i8 + (isq ? 1152 * s + 128 * ql0 : fcol) - i176;
#define DMA_A(i, lds) dma16s<(i) * 1024>(sA, voA, so, (i) == 0 ? i176 : i8, lds)

  u32x4 xr[18];
  u32x2 ep; u32 braw;
  {
    const u32x4 sX = make_srd(x, 2048 * 1152);
    const u32 vo = (l >> 2) * 1152 + p4 * 16;
#define XL(i) xr[i] = ld16<64 * (i)>(sX, vo, xso);
    XL(0) XL(1) XL(2) XL(3) XL(4) XL(5) XL(6) XL(7) XL(8) XL(9) XL(10) XL(11) XL(12) XL(13) XL(14) XL(15) XL(16) XL(17)
#undef XL
    asm volatile("buffer_load_dwordx2 %0, %1, %2, 0 offen sc1" : "=v"(ep) : "v"(WS_EPOCH), "s"(sWS) : "memory");
    const int bi = isq ? tid - 96 : tid;
    asm volatile("buffer_load_dword %0, %1, %2, 0 offen" : "=v"(braw) : "v"((bi >= 0 && bi < 192) ? fcol + 4 * bi : OOB), "s"(make_srd(b1, 9216)) : "memory");
    bar();
#pragma unroll
    for (int p = 0; p < 3; ++p) { DMA_A(0, dl + p * SLOT_A); DMA_A(1, dl + p * SLOT_A); DMA_A(2, dl + p * SLOT_A); }
    {
      const int row = ((m & 7) * 8 + w) * 9 + l % 9, ln = l / 9;
      touch(sQKV, (isq && ln < 2) ? row * 3456 + 1152 * s + 128 * (ql1 + ln) : OOB, LB + L_DUMP);
      touch(sW1, ln < (isq ? 4 : 5) ? row * 9216 + fcol + 128 * (ln + (isq ? 0 : 1)) : OOB, LB + L_DUMP);
    }
  }
  if (w == 0) {
    u32x4 mk[2];
#pragma unroll
    for (int e = 0; e < 8; ++e) { const int d = (l & 31) - 4 * h - ((2 * e & 3) + 8 * (e >> 1)); mk[e >> 2][e & 3] = d < 0 ? 0u : (d == 0 ? 0xffffu : ~0u); }
    lds_w16(LB + L_MSK + l * 16, mk[0]); lds_w16(LB + L_MSK + 1024 + l * 16, mk[1]);
  }
  float nmean;
  u64 tag;
  const u32 xwa = LB + L_KV + r * 6144 + p4 * 512 + (((16 * c + (l >> 2)) ^ (p4 << 1)) << 4), xra = LB + L_KV + r * 6144 + h * 512 + (((l & 31) ^ (h << 1)) << 4);
  {
    float s0 = 0.f, s1 = 0.f, q0 = 0.f, q1 = 0.f;
#pragma unroll
    for (int T = 0; T < 6; ++T) {
      if (T == 0) wait_vm<26 + NT>(); else if (T == 1) wait_vm<23 + NT>(); else if (T == 2) wait_vm<20 + NT>(); else if (T == 3) wait_vm<17 + NT>(); else if (T == 4) wait_vm<14 + NT>(); else wait_vm<9 + NT>();
#pragma unroll
      for (int i = 3 * T; i < 3 * T + 3; ++i) {
        pin(xr[i]);
        s0 = dot2(xr[i][0], 0x3f803f80u, s0); s1 = dot2(xr[i][1], 0x3f803f80u, s1); q0 = dot2(xr[i][0], xr[i][0], q0); q1 = dot2(xr[i][1], xr[i][1], q1);
        s0 = dot2(xr[i][2], 0x3f803f80u, s0); s1 = dot2(xr[i][3], 0x3f803f80u, s1); q0 = dot2(xr[i][2], xr[i][2], q0); q1 = dot2(xr[i][3], xr[i][3], q1);
      }
      fence_sched();
    }
    STAMP(1);
    pin(ep); pin(braw);
    const float mean = quad_sum(s0 + s1) * (1.f / 576.f);
    nmean = -rbf(mean);
#pragma unroll
    for (int i = 0; i < 6; ++i) {
      float d0, d1, d2, d3, d4, d5, d6, d7;
      asm volatile("s_nop 1\n\tv_dot2_f32_bf16 %4, %0, %13, %12\n\tv_dot2_f32_bf16 %5, %0, %14, %12\n\tv_dot2_f32_bf16 %6, %1, %13, %12\n\tv_dot2_f32_bf16 %7, %1, %14, %12\n\t"
                   "v_dot2_f32_bf16 %8, %2, %13, %12\n\tv_dot2_f32_bf16 %9, %2, %14, %12\n\tv_dot2_f32_bf16 %10, %3, %13, %12\n\tv_dot2_f32_bf16 %11, %3, %14, %12\n\t"
                   "v_cvt_pk_bf16_f32 %0, %4, %5\n\tv_cvt_pk_bf16_f32 %1, %6, %7\n\tv_cvt_pk_bf16_f32 %2, %8, %9\n\tv_cvt_pk_bf16_f32 %3, %10, %11"
                   : "+v"(xr[i][0]), "+v"(xr[i][1]), "+v"(xr[i][2]), "+v"(xr[i][3]), "=&v"(d0), "=&v"(d1), "=&v"(d2), "=&v"(d3), "=&v"(d4), "=&v"(d5), "=&v"(d6), "=&v"(d7)
                   : "v"(nmean), "s"(0x00003f80u), "s"(0x3f800000u));
      lds_w16(xwa + (i / 3) * SLOT_A + (i % 3) * 2048, xr[i]);
    }
    const float var = __builtin_fmaxf(quad_sum(q0 + q1) * (1.f / 576.f) - mean * mean, 0.f);
    const float rsd = 1.f / rbf(__builtin_sqrtf(rbf(rbf(var) + 1e-5f)));
    if (p4 == 0) *(LAS float*)(LB + L_RS + (32 * r + 16 * c + (l >> 2)) * 4) = rsd;
    if (tid < 224) *(LAS f32x2*)(LB + L_BIAS + tid * 8) = f32x2{blo(braw), bhi(braw)};
    tag = (((u64)(u32)__builtin_amdgcn_readfirstlane(ep[1]) << 32) | (u32)__builtin_amdgcn_readfirstlane(ep[0])) + 1;
  }
  wait_vm<3 + NT>(); wait_lgkm0(); bar();
  STAMP(2);

  const u32x4 sG = make_srd(ws + WS_G + (m * 4 + kq) * G_SZ, G_SZ);
  const u32 gvo = 2 * r * 1024 + blk * 1024 + h * 256 + t16 * 16;
  const u32 wl = LB + h * 1024 + q4 * 128 + 64 * (c ^ ((q4 >> 1) & 1)) + 32 * blk + 8 * p4;
  constexpr int PF = 4;
  bf16x8 wf[PF];
  u32x4 xf[36];
  float rs, pre[16];
  u32 ba = LB + L_BIAS + c * 128 + h * 16;
  {
    xf[0] = lds_r16(xra);
#pragma unroll
    for (int j = 0; j < PF; ++j) wf[j] = tr_frag(wl + j * 2048, wl + j * 2048 + 512);
#pragma unroll
    for (int i = 1; i < 6; ++i) xf[i] = lds_r16((xra ^ ((i & 1) << 6)) + i * 1024);
    rs = *(LAS float*)(LB + L_RS + (32 * r + (l & 31)) * 4);
    if (isq) { voA = vo_q; so += 128 * (ql1 - ql0) - 576 * 3456; } else so += 128 - 576 * 9216;
    f32x16 acc = {};
    fence_sched();
#pragma unroll
    for (int kk = 0; kk < 36; ++kk) {
      const int u = kk / 6, ks = kk % 6;
      acc = MFMA32(wf[kk % PF], as_bf(xf[kk]), acc); pin(acc);
      if (u < 4) {
        const int i = 3 * (u + 2) + (ks >> 1), e = 2 * (ks & 1);
        u32 v0 = pk2(blo(xr[i][e]) + nmean, bhi(xr[i][e]) + nmean), v1 = pk2(blo(xr[i][e + 1]) + nmean, bhi(xr[i][e + 1]) + nmean);
        pin(v0); pin(v1); xr[i][e] = v0; xr[i][e + 1] = v1;
        if (kk == 1) { wait_lgkm0(); bar(); }
        if (ks & 1) { lds_w16(xwa + (u & 1) * SLOT_A + (ks >> 1) * 2048, xr[i]); fence_sched(); }
      }
      if (u < 5 && ks < 3) {
        xf[6 * (u + 1) + 2 * ks] = lds_r16(xra + ((u + 1) & 1) * SLOT_A + 2 * ks * 1024);
        xf[6 * (u + 1) + 2 * ks + 1] = lds_r16((xra ^ 64) + ((u + 1) & 1) * SLOT_A + (2 * ks + 1) * 1024);
      }
      { const u32 pa = wl + ((kk + PF) / 12) * SLOT_A + ((kk + PF) % 12) * 2048; wf[kk % PF] = tr_frag(pa, pa + 512); }
      if (kk % 12 == 1) DMA_A(0, dl + ((kk / 12 + 3) & 3) * SLOT_A);
      if (kk % 12 == 5) DMA_A(1, dl + ((kk / 12 + 3) & 3) * SLOT_A);
      if (kk % 12 == 9) DMA_A(2, dl + ((kk / 12 + 3) & 3) * SLOT_A);
      if ((kk >= 26 && kk < 29) || kk == 34) {
        const u32x4 sv = {SENT, SENT, SENT, SENT};
        st16c<0>(sv, sWS, (kk < 29 || tid < 192) ? l * 16 + (kk < 29 ? kk - 26 : 3) * 8192 : OOB, WS_P + bid * 27648 + w * 1024);
      }
      if (kk == 30) { const int ln = ((bid >> 3) * 8 + w) * 41 + l; touch(make_srd(w2 + 1152 * kq * 288, 1152 * 1152), l < 41 ? ln * 128 : OOB, LB + L_DUMP); }
      if (kk == 31) { const int ln = ((bid >> 3) * 8 + w) * 6 + l; touch(make_srd(wo + 144 * kq * 288, 144 * 1152), l < 6 ? ln * 128 : OOB, LB + L_DUMP); }
      if (kk >= 32) { const f32x4 b = *(LAS f32x4*)(ba + (kk - 32) * 32); pre[4 * (kk - 32)] = b[0]; pre[4 * (kk - 32) + 1] = b[1]; pre[4 * (kk - 32) + 2] = b[2]; pre[4 * (kk - 32) + 3] = b[3]; }
      fence_sched();
      if (kk == 35) {
        const float re = s == 0 ? rs * 0.34004557f : rs;
#pragma unroll
        for (int e = 0; e < 16; ++e) { pre[e] = __builtin_fmaf(acc[e], re, pre[e]); pin(pre[e]); }
      }
      if (ks == 5) {
        STAMP(41 + 2 * u);
        if (u < 5) asm volatile("s_waitcnt lgkmcnt(2)" ::: "memory");
        if (u == 5) wait_vm<7>(); else if (u & 1) wait_vm<3>();
        if (u != 4) bar();
        STAMP(42 + 2 * u);
      }
    }
  }
  STAMP(3);
  u32 hso;
  {
    u32 R0 = 3 * SLOT_A, R1 = 0, R2 = SLOT_A, R3 = 2 * SLOT_A;
    const u32 qvo = h * 1024 + (32 * r + (l & 31)) * 8;
    const u32 qbase = WS_QKV + ((m * 4 + kq) * 3 + s) * 36864;
#pragma unroll 1
    for (int ci = 1; ci < NC; ++ci) {
      STAMP(3 + ci);
      ba += 256;
      const bool pq = isq && ci < 4;
      const float k1 = pq ? 0.f : GK1, k2 = pq ? 0.f : GK2, zb = pq ? -200.f : 0.f;
      const int cl0 = (ci == 1 ? 64 * ql0 + 32 * (kq < 2 ? c : c ^ 1) : 64 * (ql1 + ci - 2) + 32 * c) - 144 * kq;
      const u32 o0 = pq ? ((u32)cl0 < 144u ? qbase + cl0 * 256 : OOB) : (8 * s + 2 * (ci - (isq ? 4 : 1)) + c) * 8192;
      const u32 o1 = (u32)(cl0 + 16) < 144u ? qbase + (cl0 + 16) * 256 : OOB;
      if (isq && ci == 2) { sA = sW1; voA = vo_f; i8 = 8 * 9216; i176 = 176 * 9216; so = 24 * w * 9216 + fcol - i176; }
      else so += 128 - 72 * i8;
      if (ci == NC - 1) sA[2] = 0;
      if (ci == 5 && tid == 0) __hip_atomic_store((GAS u64*)(ws + WS_FLAG + bid * 128), tag, RLX_AGENT);
      f32x16 acc = {};
      u32x4 un[2];
      float ea, eb;
#pragma unroll
      for (int sl = 0; sl < 3; ++sl) {
        const u32 cur = wl + (sl == 0 ? R0 : sl == 1 ? R1 : R2), nxt = wl + (sl == 0 ? R1 : sl == 1 ? R2 : R3);
        const u32 dst = dl + (sl == 0 ? R3 : sl == 1 ? R0 : R1);
#pragma unroll
        for (int ks = 0; ks < 12; ++ks) {
          const int kk = 12 * sl + ks;
          acc = MFMA32(wf[ks % PF], as_bf(xf[kk]), acc); pin(acc);
          const u32 pa = (ks + PF < 12 ? cur : nxt) + ((ks + PF) % 12) * 2048;
          wf[ks % PF] = tr_frag(pa, pa + 512);
          if (ks == 1) DMA_A(0, dst);
          if (ks == 5) DMA_A(1, dst);
          if (ks == 9) DMA_A(2, dst);
          if (kk < 32) {
            const int e = 2 * (kk >> 2);
            if ((kk & 3) == 0) { ea = gelu_a(pre[e], k1, k2, zb); pin(ea); }
            if ((kk & 3) == 1) { eb = gelu_a(pre[e + 1], k1, k2, zb); pin(eb); }
            if ((kk & 3) == 2) { ea = gelu_b(pre[e], ea); pin(ea); }
            if ((kk & 3) == 3) { u32 v = pk2(ea, gelu_b(pre[e + 1], eb)); pin(v); un[kk >> 4][(kk >> 2) & 3] = v; }
          } else {
            const f32x4 b = *(LAS f32x4*)(ba + (kk - 32) * 32); pre[4 * (kk - 32)] = b[0]; pre[4 * (kk - 32) + 1] = b[1]; pre[4 * (kk - 32) + 2] = b[2]; pre[4 * (kk - 32) + 3] = b[3];
          }
          if (kk == 22 || kk == 34) {
            const u32x4 v = un[kk == 34];
            if (pq) { st8c<0>(u32x2{v[0], v[1]}, sWS, qvo, kk == 34 ? o1 : o0); st8c<2048>(u32x2{v[2], v[3]}, sWS, qvo, kk == 34 ? o1 : o0); }
            else if (kk == 34) st16c<512>(v, sG, gvo, o0); else st16c<0>(v, sG, gvo, o0);
          }
          if (kk == 24 && ci == 4) wait_vm<0>();
          fence_sched();
        }
        if (sl == 2) {
          const float re = (s == 0 && ci < 3) ? rs * 0.34004557f : rs;
#pragma unroll
          for (int e = 0; e < 16; ++e) { pre[e] = __builtin_fmaf(acc[e], re, pre[e]); pin(pre[e]); }
          const u32 t = R3; R3 = R2; R2 = R1; R1 = R0; R0 = t;
        }
        if (sl == 2) wait_vm<5>(); else wait_vm<4>();
        bar();
      }
    }
    hso = (8 * s + 2 * (NC - (isq ? 4 : 1)) + c) * 8192;
  }
  STAMP(10);
  const auto rWS = __builtin_amdgcn_make_buffer_rsrc(ws, 0, WS_END, 0x00020000);
  u32x4 qf; u32 qtl;
  {
    const int pm = l < 3 ? m : (l < 5 ? m - 1 : m - 2), ps = l < 3 ? l : 1 + (l & 1);
    poll_words(ws, WS_FLAG + bid_of(pm, kq, ps) * 128, l < 7 && pm >= 0, tag);
#pragma unroll
    for (int i = 0; i < 7; ++i) {
      const int n = w + 8 * i, mt = n / 18, pl = n % 18;
      const int sm_ = m - 2 + mt;
      const u32 src = sm_ >= 0 ? WS_QKV + (sm_ * 4 + kq) * QKV_SZ + (pl < 9 ? 36 + pl : 63 + pl) * 1024 + 9216 * s : OOB;
      if (i < 6 || w < 6) dma16c<0, 0>(sWS, l * 16, src, LB + L_KV + pl * PP + mt * 1024);
    }
    const u32 qo = WS_QKV + (m * 4 + kq) * QKV_SZ + 9216 * s, qv = (32 * r + (l & 31)) * 8;
    qf = cat(__builtin_amdgcn_raw_buffer_load_b64(rWS, qv + (5 * c + 2 * h) * 1024, qo, 16), __builtin_amdgcn_raw_buffer_load_b64(rWS, qv + (5 * c + 2 * h + 1) * 1024, qo, 16));
    qtl = __builtin_amdgcn_raw_buffer_load_b32(rWS, qv + 4096 + 4 * c, qo, 16);
    if (tid < 384) *(LAS u32x2*)(LB + L_KV + 18 * PP + tid * 8) = u32x2{tid < 256 - 128 * m ? 0xff7f3f80u : 0x00003f80u, 0x3f803f80u};
#pragma unroll
    for (int u = 0; u < 2; ++u) {
      u32x4 un;
#pragma unroll
      for (int e = 0; e < 4; ++e) un[e] = pk2(gelu_b(pre[8 * u + 2 * e], gelu_a(pre[8 * u + 2 * e], GK1, GK2, 0.f)), gelu_b(pre[8 * u + 2 * e + 1], gelu_a(pre[8 * u + 2 * e + 1], GK1, GK2, 0.f)));
      pin(un);
      if (u == 0) { pin(qf); pin(qtl); }
      if (u) st16c<512>(un, sG, gvo, hso); else st16c<0>(un, sG, gvo, hso);
    }
    wait_vm<2>(); wait_lgkm0(); bar();
  }
  if (bid == 24 && w == 4) {
#pragma unroll 1
    for (int i = 0; i < 4; ++i) poll_words(ws, WS_FLAG + (l + 64 * i) * 128, true, tag);
    if (l == 0) __hip_atomic_store((GAS u64*)(ws + WS_EPOCH), tag, RLX_AGENT);
  }
  STAMP(11);
  u32 vb[3];
#pragma unroll
  for (int i = 0; i < 3; ++i) { const int pc = 64 * (w + 8 * i) + l; vb[i] = (pc / 18) * 1152 + (pc % 18) * 16 + 288 * s; }
  const u64 m3 = __builtin_amdgcn_ballot_w64(tid < 128);
  u32x4 sB = make_srd(w2 + 1152 * kq * 288, 1152 * 1152);
  const auto rG = __builtin_amdgcn_make_buffer_rsrc(ws + WS_G + (m * 4 + kq) * G_SZ, 0, G_SZ, 0x00020000);
  u32 woff = 4 * s * 73728, goff = 4 * s * 16384, wwrap = 18 * 73728;
  const u32 db = LB + w * 1024;
  const u32 gl = c * 8192 + 2 * r * 1024 + l * 16;
  u32x4 gf[7][2];
#define ISSUE_W(i, rbo) do { if ((i) == 0) dma16<0, 0>(sB, vb[0], woff, db + (rbo)); else if ((i) == 1) dma16<8192, 0>(sB, vb[1], woff, db + (rbo)); \
                             else { dma16m<16384>(sB, vb[2], woff, db + (rbo), m3); woff += 73728; if (woff == wwrap) woff = 0; } } while (0)
#define ISSUE_G(e, slot) do { gf[slot][e] = __builtin_amdgcn_raw_buffer_load_b128(rG, gl + (e) * 1024, goff, 16); if (e) { goff += 16384; if (woff == 0) goff = 0; } } while (0)
  f32x4 ac[2][9];
  {
    const u32 kr = LB + L_KV + (32 * r + (l & 31)) * 8, ka = kr + (5 * c + 2 * h) * PP;
    u32x4 qt = {};
    qt[0] = (h || c) ? 0u : qtl; qt[1] = (h || !c) ? 0u : qtl; qt[2] = h ? 0u : 0x3f800000u;
    f32x16 st[9];
    const f32x16 zero = {};
    u32x4 kt8;
#pragma unroll
    for (int i = 0; i < 9; ++i) {
      const u32x4 kt = cat(lds_r8(kr + 4 * PP + i * 256), lds_r8(kr + 18 * PP + i * 256));
      st[i] = MFMA32(as_bf(cat(lds_r8(ka + i * 256), lds_r8(ka + PP + i * 256))), as_bf(qf), zero);
      st[i] = MFMA32(as_bf(kt), as_bf(qt), st[i]);
      if (i == 8) kt8 = kt;
      if (i < 6) ISSUE_W(i % 3, (i / 3) * SLOT_B);
    }
#pragma unroll
    for (int e = 0; e < 16; ++e) st[8][e] = __builtin_amdgcn_inverse_ballot_w64(diag_mask(e)) ? st[8][e] : st[0][e];
    float mx0 = st[1][0], mx1 = st[1][1];
#pragma unroll
    for (int i = 1; i < 9; ++i) {
#pragma unroll
      for (int e = (i == 1 ? 2 : 0); e < 16; e += 2) { if (e & 2) mx0 = max3(mx0, st[i][e], st[i][e + 1]); else mx1 = max3(mx1, st[i][e], st[i][e + 1]); }
    }
    float mx;
    { auto sw = __builtin_amdgcn_permlane32_swap(f2u(mx0), f2u(mx1), false, false); mx = max3(u2f(sw[0]), u2f(sw[1]), u2f(sw[0]));
      auto s2 = __builtin_amdgcn_permlane32_swap(f2u(mx), f2u(mx), false, false); mx = max3(u2f(s2[0]), u2f(s2[1]), u2f(s2[0])); }
    u32x4 qm = {};
    { const float n0 = -mx, a0 = bhi(f2u(n0)), n1 = n0 - a0, a1 = bhi(f2u(n1)), n2 = n1 - a1;
      qm[2] = h ? 0u : f2u(a0) >> 16; qm[3] = h ? 0u : ((f2u(a1) >> 16) | (f2u(n2) & 0xffff0000u)); }
#pragma unroll
    for (int i = 1; i < 9; ++i) st[i] = MFMA32(as_bf(kt8), as_bf(qm), st[i]);
    STAMP(12);
    fence_sched(); wait_vm<0>(); bar();
    STAMP(13);
    if (tid == 0) __hip_atomic_store((GAS u64*)(ws + WS_FLAG + (256 + bid) * 128), tag, RLX_AGENT);
    const u32 va = LB + L_KV + (32 * r + 4 * h + q4) * 8 + (blk ? (p4 ? 18 : 13) : 9 + 5 * c + p4) * PP;
    f32x16 o = {};
#pragma unroll
    for (int i = 1; i < 9; ++i) {
#pragma unroll
      for (int u = 0; u < 2; ++u) {
        u32x4 pf;
#pragma unroll
        for (int e = 0; e < 4; ++e) pf[e] = pk2(__builtin_amdgcn_exp2f(st[i][8 * u + 2 * e]), __builtin_amdgcn_exp2f(st[i][8 * u + 2 * e + 1]));
        if (i == 8) {
          const u32x4 mk = lds_r16(LB + L_MSK + u * 1024 + l * 16);
          o = MFMA32(tr_frag(va + u * 128, va + u * 128 + 64), as_bf(pf & ~mk), o);
          pf &= mk;
        }
        o = MFMA32(tr_frag(va + i * 256 + u * 128, va + i * 256 + u * 128 + 64), as_bf(pf), o); pin(o);
        const int j = 2 * (i - 1) + u;
        if (j < 8) gf[j >> 1][j & 1] = __builtin_amdgcn_raw_buffer_load_b128(rG, gl + (j & 1) * 1024, goff + (j >> 1) * 16384, 16);
        else if (j < 14) ISSUE_W((j - 8) % 3, (2 + (j - 8) / 3) * SLOT_B);
        fence_sched();
      }
    }
    goff += 4 * 16384;
    const float inv = 1.f / (h ? o[8] : o[12]);
    auto qoff = [&](int Q) -> u32 { return (36 + (Q >> 3)) * 8192 + (((Q >> 2) & 1) * 2 + (Q & 1)) * 256 + ((Q >> 1) & 1) * 8; };
    const u32 tko = (2 * r + blk) * 1024 + t16 * 16;
    st8c<0>(u32x2{pk2(o[0] * inv, o[1] * inv), pk2(o[2] * inv, o[3] * inv)}, sG, tko + qoff(9 * s + 5 * c + h), 0);
    st8c<0>(u32x2{pk2(o[4] * inv, o[5] * inv), pk2(o[6] * inv, o[7] * inv)}, sG, tko + qoff(9 * s + 5 * c + 2 + h), 0);
    st4c(c ? pk2(o[10] * inv, o[11] * inv) : pk2(o[8] * inv, o[9] * inv), sG, h ? OOB : tko + 4 * c, qoff(9 * s + 4));
    st16c<0>(u32x4{0u, 0u, 0u, 0u}, sG, tko + (2 + h) * 256, (s == 3 && c == 0) ? 40 * 8192 : OOB);
  }
  STAMP(14);
#pragma unroll
  for (int e = 0; e < 2; ++e)
#pragma unroll
    for (int d = 0; d < 9; ++d) ac[e][d] = f32x4{};
  {
    const u32 wb = LB + c * 9216 + (16 * (g4 >> 1) + 4 * (g4 & 1) + q4) * 288 + 8 * p4;
    poll_words(ws, WS_FLAG + (256 + bid_of(m, kq, l & 3)) * 128, l < 4, tag);
    wait_vm<0>(); bar();
    if (tid == 0) __hip_atomic_store((GAS u64*)(ws + WS_FLAG + (512 + bid) * 128), tag, RLX_AGENT);
    dma16<L_KV, 0>(make_srd(x, (128 * m + 32 * kq + 32) * 1152), vb[0], (128 * m + 32 * kq) * 1152, db);
    if (w == 0) { dma16<L_KV + 8192, 0>(make_srd(x, (128 * m + 32 * kq + 32) * 1152), vb[1], (128 * m + 32 * kq) * 1152, db); dma16<L_KV + 9216, 0>(make_srd(b2, 1152), vb[0], 0, db); }
    bf16x8 af[9];
#pragma unroll
    for (int d = 0; d < 9; ++d) af[d] = tr_frag(wb + d * 32, wb + d * 32 + 2304);
    u32 rbn = SLOT_B, rbf_ = 4 * SLOT_B;
    STAMP(15);
#pragma unroll 1
    for (int a = 0; a < 3; ++a) {
      if (a == 2) {
        poll_words(ws, l < 4 ? WS_FLAG + (512 + bid_of(m, kq, l & 3)) * 128 : WS_FLAG + bid_of(m, l & 3, s) * 128, l < 8, tag);
        sB = make_srd(wo + 144 * kq * 288, (kq == 3 ? 144 : 160) * 1152);
        woff = 0; goff = 18 * 16384;
      }
#pragma unroll
      for (int b = 0; b < 7; ++b) {
        const u32 nxt = wb + rbn;
#pragma unroll
        for (int d = 0; d < 9; ++d) {
          ac[0][d] = MFMA16(af[d], as_bf(gf[b][0]), ac[0][d]);
          ac[1][d] = MFMA16(af[d], as_bf(gf[b][1]), ac[1][d]);
          pin(ac[0][d]); pin(ac[1][d]);
          af[d] = tr_frag(nxt + d * 32, nxt + d * 32 + 2304);
          if (d == 0) ISSUE_W(0, rbf_);
          if (d == 2) ISSUE_G(0, (b + 4) % 7);
          if (d == 3) ISSUE_W(1, rbf_);
          if (d == 5) ISSUE_W(2, rbf_);
          if (d == 6) ISSUE_G(1, (b + 4) % 7);
          if (d == 7) { rbf_ = rbf_ == 4 * SLOT_B ? 0 : rbf_ + SLOT_B; rbn = rbn == 4 * SLOT_B ? 0 : rbn + SLOT_B; }
          fence_sched();
        }
        wait_vm<8>();
        bar();
      }
      STAMP(16 + a);
    }
  }
  {
    wait_vm<0>(); wait_lgkm0(); bar();
    const u32 xa = LB + l * 16;
#pragma unroll
    for (int d = 0; d < 9; ++d) *(LAS f32x4*)(xa + w * 9216 + d * 1024) = c ? ac[0][d] : ac[1][d];
    wait_lgkm0(); bar();
    f32x4 v[9];
#pragma unroll
    for (int d = 0; d < 9; ++d) v[d] = (c ? ac[1][d] : ac[0][d]) + *(LAS f32x4*)(xa + (w ^ 4) * 9216 + d * 1024);
    STAMP(19);
    if (r != kq) {
      u32 pkd[18];
#pragma unroll
      for (int d = 0; d < 9; ++d) { pkd[2 * d] = umin(pk2(v[d][0], v[d][1]), SENT - 1); pkd[2 * d + 1] = umin(pk2(v[d][2], v[d][3]), SENT - 1); }
      const u32 po = WS_P + bid_of(m, r, s) * 27648 + ((kq - r - 1) & 3) * 9216 + c * 4608;
      st16c<0>(u32x4{pkd[0], pkd[1], pkd[2], pkd[3]}, sWS, l * 16, po); st16c<1024>(u32x4{pkd[4], pkd[5], pkd[6], pkd[7]}, sWS, l * 16, po);
      st16c<2048>(u32x4{pkd[8], pkd[9], pkd[10], pkd[11]}, sWS, l * 16, po); st16c<3072>(u32x4{pkd[12], pkd[13], pkd[14], pkd[15]}, sWS, l * 16, po);
      st8c<0>(u32x2{pkd[16], pkd[17]}, sWS, l * 8 + 4096, po);
    } else {
      const u32 ra = LB + L_KV + (16 * c + t16) * 288 + g4 * 8;
#pragma unroll
      for (int d = 0; d < 9; ++d) {
        const u32x2 xv = lds_r8(ra + 32 * d), bv = lds_r8(LB + L_KV + 9216 + g4 * 8 + 32 * d);
        v[d] += f32x4{blo(xv[0]), bhi(xv[0]), blo(xv[1]), bhi(xv[1])} + f32x4{blo(bv[0]), bhi(bv[0]), blo(bv[1]), bhi(bv[1])};
      }
      const u32 po = WS_P + bid * 27648 + c * 4608;
      u32x4 pa[3][4]; u32x2 pb[3];
#pragma unroll 1
      for (u32 spins = 0; spins < (1u << 18); ++spins) {
        asm volatile("" ::: "memory");
#pragma unroll
        for (int sg = 0; sg < 3; ++sg) {
#pragma unroll
          for (int i = 0; i < 4; ++i) pa[sg][i] = __builtin_amdgcn_raw_buffer_load_b128(rWS, l * 16 + i * 1024, po + sg * 9216, 16);
          pb[sg] = __builtin_amdgcn_raw_buffer_load_b64(rWS, l * 8 + 4096, po + sg * 9216, 16);
        }
        u32 mxu = 0;
#pragma unroll
        for (int sg = 0; sg < 3; ++sg) {
#pragma unroll
          for (int i = 0; i < 4; ++i) mxu = umax(umax(mxu, umax(pa[sg][i][0], pa[sg][i][1])), umax(pa[sg][i][2], pa[sg][i][3]));
          mxu = umax(mxu, umax(pb[sg][0], pb[sg][1]));
        }
        if (all_lanes(mxu != SENT)) break;
        __builtin_amdgcn_s_sleep(1);
      }
      STAMP(20);
#pragma unroll
      for (int d = 0; d < 9; ++d) {
        f32x4 t = v[d];
#pragma unroll
        for (int sg = 0; sg < 3; ++sg) {
          const u32 d0 = d < 8 ? pa[sg][d >> 1][2 * (d & 1)] : pb[sg][0], d1 = d < 8 ? pa[sg][d >> 1][2 * (d & 1) + 1] : pb[sg][1];
          t += f32x4{blo(d0), bhi(d0), blo(d1), bhi(d1)};
        }
        *(LAS u32x2*)(ra + 32 * d) = u32x2{pk2(t[0], t[1]), pk2(t[2], t[3])};
      }
      const u32x4 sOUT = make_srd(out, 2048 * 1152);
#pragma unroll
      for (int i = 0; i < 5; ++i) {
        const int pc = 64 * i + l;
        const u32x4 t = lds_r16(LB + L_KV + c * 4608 + (pc < 288 ? pc : 0) * 16);
        asm volatile("buffer_store_dwordx4 %0, %1, %2, %3 offen\n\ts_nop 1" :: "v"(t), "v"(pc < 288 ? (u32)((pc / 18) * 1152 + (pc % 18) * 16 + 288 * s) : OOB), "s"(sOUT), "s"(xso) : "memory");
      }
    }
  }
}
#if defined(__HIP_DEVICE_COMPILE__)
#pragma clang attribute pop
#endif

extern "C" void kernel_launch(void* const* d_in, const int* in_sizes, int n_in, void* d_out, int out_size, void* d_ws, size_t ws_size, hipStream_t stream) {
  fused_block<<<256, 512, 0, stream>>>((const u32*)d_in[0], (const u32*)d_in[1], (const u32*)d_in[2], (const u32*)d_in[4], (const u32*)d_in[5],
                                       (const u32*)d_in[6], (const u32*)d_in[7], (u32*)d_out, (char*)d_ws);
}
```

```cpp
#include <hip/hip_runtime.h>
#pragma clang diagnostic ignored "-Wint-to-pointer-cast"
#if defined(__HIP_DEVICE_COMPILE__)
#pragma clang attribute push (__attribute__((target("no-packed-fp32-ops"))), apply_to = function)
#endif
typedef unsigned u32;
typedef unsigned long long u64;
typedef short s16x4 __attribute__((ext_vector_type(4)));
typedef __bf16 bf16x8 __attribute__((ext_vector_type(8)));
typedef __bf16 bf16x2 __attribute__((ext_vector_type(2)));
typedef float f32x2 __attribute__((ext_vector_type(2)));
typedef float f32x4 __attribute__((ext_vector_type(4)));
typedef float f32x16 __attribute__((ext_vector_type(16)));
typedef u32 u32x2 __attribute__((ext_vector_type(2)));
typedef u32 u32x4 __attribute__((ext_vector_type(4)));
#define LAS __attribute__((address_space(3)))
#define GAS __attribute__((address_space(1)))
#define DEV __device__ __forceinline__
#define RLX_AGENT __ATOMIC_RELAXED, __HIP_MEMORY_SCOPE_AGENT

constexpr u32 WS_EPOCH = 0;
constexpr u32 WS_FLAG = 1024;
constexpr u32 QKV_SZ = 3 * 36 * 1024;
constexpr u32 WS_QKV = 131072;
constexpr u32 G_SZ = 42 * 8192;
constexpr u32 WS_G = WS_QKV + 64 * QKV_SZ;
constexpr u32 WS_P = WS_G + 64 * G_SZ;
constexpr u32 WS_END = WS_P + 256 * 27648;
constexpr u32 OOB = 0x7ffffff0u;
constexpr u32 PP = 3104;
constexpr u32 L_KV = 98304, L_BIAS = 157312, L_RS = 159360, L_MSK = 159872, L_DUMP = 161920, L_FLG = 162176, L_TOTAL = 163840;
constexpr u32 SLOT_A = 24576, SLOT_B = 18432, L_RES = L_KV + 43008;
constexpr u32 SENT = 0xffffffffu;

DEV u32x4 make_srd(const void* p, u32 bytes) { u64 a = (u64)p; return u32x4{(u32)a, (u32)(a >> 32) & 0xffffu, bytes, 0x00020000u}; }
template <int LI, int SI> DEV void dma16(u32x4 srd, u32 voff, u32 soff, u32 lds) {
  u32 t;
  asm volatile("s_add_u32 m0, %1, %5\n\ts_add_u32 %0, %4, %6\n\tbuffer_load_dwordx4 %2, %3, %0 offen lds"
               : "=&s"(t) : "s"(lds), "v"(voff), "s"(srd), "s"(soff), "i"(LI), "i"(SI) : "memory", "scc");
}
template <int LI, int SI> DEV void dma16c(u32x4 srd, u32 voff, u32 soff, u32 lds) {
  u32 t;
  asm volatile("s_add_u32 m0, %1, %5\n\ts_add_u32 %0, %4, %6\n\tbuffer_load_dwordx4 %2, %3, %0 offen sc1 lds"
               : "=&s"(t) : "s"(lds), "v"(voff), "s"(srd), "s"(soff), "i"(LI), "i"(SI) : "memory", "scc");
}
template <int LI> DEV void dma16s(u32x4 srd, u32 voff, u32& so, u32 inc, u32 lds) {
  asm volatile("s_add_u32 m0, %1, %5\n\ts_add_u32 %0, %0, %4\n\tbuffer_load_dwordx4 %2, %3, %0 offen lds"
               : "+s"(so) : "s"(lds), "v"(voff), "s"(srd), "s"(inc), "i"(LI) : "memory", "scc");
}
DEV void touch(u32x4 srd, u32 voff, u32 lds) {
  asm volatile("s_mov_b32 m0, %0\n\ts_nop 0\n\tbuffer_load_dword %1, %2, 0 offen lds" :: "s"(lds), "v"(voff), "s"(srd) : "memory");
}
template <int LI, int SI> DEV void dma16m(u32x4 srd, u32 voff, u32 soff, u32 lds, u64 mask) {
  u32 t;
  asm volatile("s_add_u32 m0, %1, %6\n\ts_add_u32 %0, %4, %7\n\ts_mov_b64 exec, %5\n\tbuffer_load_dwordx4 %2, %3, %0 offen lds\n\ts_mov_b64 exec, -1"
               : "=&s"(t) : "s"(lds), "v"(voff), "s"(srd), "s"(soff), "s"(mask), "i"(LI), "i"(SI) : "memory", "scc");
}
template <int OFF> DEV u32x4 ld16(u32x4 srd, u32 voff, u32 soff) {
  u32x4 r; asm volatile("buffer_load_dwordx4 %0, %1, %2, %3 offen offset:%4" : "=v"(r) : "v"(voff), "s"(srd), "s"(soff), "i"(OFF) : "memory"); return r;
}
template <int OFF> DEV void st16c(u32x4 v, u32x4 srd, u32 voff, u32 soff) {
  asm volatile("buffer_store_dwordx4 %0, %1, %2, %3 offen offset:%4 sc1\n\ts_nop 1" :: "v"(v), "v"(voff), "s"(srd), "s"(soff), "i"(OFF) : "memory");
}
template <int OFF> DEV void st8c(u32x2 v, u32x4 srd, u32 voff, u32 soff) {
  asm volatile("buffer_store_dwordx2 %0, %1, %2, %3 offen offset:%4 sc1\n\ts_nop 1" :: "v"(v), "v"(voff), "s"(srd), "s"(soff), "i"(OFF) : "memory");
}
DEV void st4c(u32 v, u32x4 srd, u32 voff, u32 soff) {
  asm volatile("buffer_store_dword %0, %1, %2, %3 offen sc1\n\ts_nop 1" :: "v"(v), "v"(voff), "s"(srd), "s"(soff) : "memory");
}
template <int OFF> DEV void st8(u32x2 v, u32x4 srd, u32 voff, u32 soff) {
  asm volatile("buffer_store_dwordx2 %0, %1, %2, %3 offen offset:%4\n\ts_nop 1" :: "v"(v), "v"(voff), "s"(srd), "s"(soff), "i"(OFF) : "memory");
}
template <class T> DEV void pins(T& v) { asm volatile("" : "+s"(v)); }
template <class T> DEV void pin(T& v) { asm volatile("" : "+v"(v)); }
#define STAMP(i)
#define STAMPR(i)
template <int N> DEV void wait_vm() { asm volatile("s_waitcnt vmcnt(%0)" :: "i"(N) : "memory"); }
DEV void wait_lgkm0() { asm volatile("s_waitcnt lgkmcnt(0)" ::: "memory"); }
DEV void fence_sched() { __builtin_amdgcn_sched_barrier(0); }
DEV void bar() { fence_sched(); __builtin_amdgcn_s_barrier(); fence_sched(); }

DEV float u2f(u32 v) { return __builtin_bit_cast(float, v); }
DEV u32 f2u(float v) { return __builtin_bit_cast(u32, v); }
DEV bf16x8 as_bf(u32x4 v) { return __builtin_bit_cast(bf16x8, v); }
DEV u32 pk2(float a, float b) { return __builtin_bit_cast(u32, __builtin_convertvector(f32x2{a, b}, bf16x2)); }
DEV float blo(u32 d) { return u2f(d << 16); }
DEV float bhi(u32 d) { return u2f(d & 0xffff0000u); }
DEV float rbf(float f) { return blo(pk2(f, f)); }
DEV float max3(float a, float b, float c) { return __builtin_elementwise_maximum(__builtin_elementwise_maximum(a, b), c); }
DEV u32 umax(u32 a, u32 b) { return a > b ? a : b; }
DEV u32 umin(u32 a, u32 b) { return a < b ? a : b; }
DEV bool all_lanes(bool p) { return __builtin_amdgcn_ballot_w64(!p) == 0; }
DEV float dot2(u32 a, u32 b, float c) { return __builtin_amdgcn_fdot2_f32_bf16(__builtin_bit_cast(bf16x2, a), __builtin_bit_cast(bf16x2, b), c, false); }
DEV float quad_sum(float v) {
  v += u2f(__builtin_amdgcn_mov_dpp(f2u(v), 0xb1, 0xf, 0xf, true));
  return v + u2f(__builtin_amdgcn_mov_dpp(f2u(v), 0x4e, 0xf, 0xf, true));
}
DEV u32x2 lds_r8(u32 a) { return *(volatile LAS u32x2*)a; }
DEV u32x4 cat(u32x2 a, u32x2 b) { return u32x4{a[0], a[1], b[0], b[1]}; }
DEV u32x4 lds_r16(u32 a) { return *(LAS u32x4*)a; }
DEV void lds_w16(u32 a, u32x4 v) { *(LAS u32x4*)a = v; }
DEV bf16x8 tr_frag(u32 a0, u32 a1) {
  s16x4 lo = __builtin_amdgcn_ds_read_tr16_b64_v4i16((LAS s16x4*)a0);
  s16x4 hi = __builtin_amdgcn_ds_read_tr16_b64_v4i16((LAS s16x4*)a1);
  return __builtin_bit_cast(bf16x8, __builtin_shufflevector(lo, hi, 0, 1, 2, 3, 4, 5, 6, 7));
}
template <int I> struct IC { static constexpr int v = I; };
template <int I, int N, class F> DEV void sfor(F&& f) { if constexpr (I < N) { f(IC<I>{}); sfor<I + 1, N>(f); } }
#define SPINS (1u << 18)
#define RARE(x) __builtin_expect(!!(x), 0)
#define MFMA32(a, b, c) __builtin_amdgcn_mfma_f32_32x32x16_bf16((a), (b), (c), 0, 0, 0)
#define MFMA16(a, b, c) __builtin_amdgcn_mfma_f32_16x16x32_bf16((a), (b), (c), 0, 0, 0)

DEV float gelu_a(float x) { return __builtin_amdgcn_exp2f(x * __builtin_fmaf(x * x, -0.10294324f, -2.3022082f)); }
DEV float gelu_b(float x, float e) { return x * __builtin_amdgcn_rcpf(1.f + e); }
DEV void poll_words(const char* ws, u32 off, bool active, u64 tag) {
#pragma unroll 1
  for (u32 spins = 0; spins < SPINS; ++spins) {
    u64 v = active ? __hip_atomic_load((GAS u64*)(ws + off), RLX_AGENT) : tag;
    if (all_lanes(v == tag)) break;
    __builtin_amdgcn_s_sleep(2);
  }
  __builtin_amdgcn_fence(__ATOMIC_ACQUIRE, "wavefront");
}
DEV int bid_of(int m, int kq, int s) { return ((m & 7) * 4 + s) * 8 + (m >> 3) * 4 + kq; }
constexpr u64 diag_mask(int e) {
  u64 mk = 0;
  for (int l = 0; l < 64; ++l) if ((e & 3) + 8 * (e >> 2) + 4 * (l >> 5) <= (l & 31)) mk |= 1ull << l;
  return mk;
}

__global__ __launch_bounds__(512, 2) void fused_block(const u32* __restrict__ x, const u32* __restrict__ wqkv, const u32* __restrict__ wo,
                                                      const u32* __restrict__ w1, const u32* __restrict__ b1, const u32* __restrict__ w2,
                                                      const u32* __restrict__ b2, u32* __restrict__ out, char* __restrict__ ws) {
  __shared__ __attribute__((aligned(1024))) char smem[L_TOTAL];
  const u32 LB = (u32)(size_t)(LAS char*)smem;
  const int tid = __builtin_amdgcn_workitem_id_x(), l = tid & 63;
  const int w = __builtin_amdgcn_readfirstlane(tid >> 6) & 7, r = w & 3, c = w >> 2;
  const int bid = __builtin_amdgcn_workgroup_id_x(), kq = bid & 3, mh = (bid >> 2) & 1, m = 8 * mh + (bid >> 5), s = (bid >> 3) & 3;
  STAMPR(62); STAMP(0);
  const int h = l >> 5, t16 = l & 15, g4 = l >> 4, q4 = (l & 15) >> 2, p4 = l & 3, blk = (l >> 4) & 1;
  const u32 xso = (128 * m + 32 * r + 16 * c) * 1152;
  const u32x4 sWS = make_srd(ws, WS_END);
  u32x4 xr[18];
  u32x2 ep; u32 braw;
  {
    const u32x4 sX = make_srd(x, 2048 * 1152);
    const u32 vo = (l >> 2) * 1152 + p4 * 16;
#define XL(i) xr[i] = ld16<64 * (i)>(sX, vo, xso);
    XL(0) XL(1) XL(2) XL(3) XL(4) XL(5) XL(6) XL(7) XL(8) XL(9) XL(10) XL(11) XL(12) XL(13) XL(14) XL(15) XL(16) XL(17)
#undef XL
    asm volatile("buffer_load_dwordx2 %0, %1, %2, 0 offen sc1" : "=v"(ep) : "v"(WS_EPOCH), "s"(sWS) : "memory");
    asm volatile("buffer_load_dword %0, %1, %2, 0 offen" : "=v"(braw) : "v"(tid < 192 ? 2304 * kq + 512 * s + 4 * tid : OOB), "s"(make_srd(b1, 9216)) : "memory");
  }
  STAMP(21);
  const bool isq = s < 3;
  const int NC = isq ? 7 : 6, NQ = isq ? 3 : 0;
  const u32x4 sW1 = make_srd(w1, 576 * 9216);
  const u32 pcs = 16 * ((l & 7) ^ (4 * blk));
  const u32 vo_f = (l >> 3) * 9216 + pcs;
  u32 dl = LB + w * 3072;
  const u32 fcol = 2304 * kq + 512 * s;
  u32x4 sA = isq ? make_srd(wqkv, 576 * 3456) : sW1;
  u32 voA = isq ? (l >> 3) * 3456 + pcs : vo_f, i8 = isq ? 8 * 3456 : 8 * 9216, i176 = 22 * i8;
  u32 so = 3 * w * i8 + (isq ? 1152 * s + 256 * kq : fcol) - i176;
#define DMA_A(i, lds) dma16s<(i) * 1024>(sA, voA, so, (i) == 0 ? i176 : i8, lds)

  if (w == 0) {
    u32x4 mk[2];
#pragma unroll
    for (int e = 0; e < 8; ++e) { const int d = (l & 31) - 4 * h - ((2 * e & 3) + 8 * (e >> 1)); mk[e >> 2][e & 3] = d < 0 ? 0u : (d == 0 ? 0xffffu : ~0u); }
    lds_w16(LB + L_MSK + l * 16, mk[0]); lds_w16(LB + L_MSK + 1024 + l * 16, mk[1]);
  }
  u32 tv0, tv1;
  {
    const int trow = ((m & 7) * 8 + w) * 9 + l % 9, ln = l / 9;
    tv0 = ln < 2 ? trow * (i8 >> 3) + (isq ? 1152 * s + 256 * kq : fcol) + 128 * (1 + ln) : OOB;
    tv1 = ln < (isq ? 4 : 3) ? trow * 9216 + fcol + 128 * (ln + (isq ? 0 : 3)) : OOB;
    pin(tv0); pin(tv1);
  }
  u64 tag;
  u32x4 xf[36];
  float rs;
  {
    u32 xwa = LB + 3 * SLOT_A + r * 18432 + p4 * 512 + (((16 * c + (l >> 2)) ^ (p4 << 1)) << 4), xra = LB + 3 * SLOT_A + r * 18432 + h * 512 + (((l & 31) ^ (h << 1)) << 4), xrb = xra ^ 64;
    pin(xwa); pin(xra); pin(xrb);
    float s0 = 0.f, s1 = 0.f, q0 = 0.f, q1 = 0.f;
    auto sq = [&](int i) { q0 = dot2(xr[i][0], xr[i][0], q0); q1 = dot2(xr[i][1], xr[i][1], q1); q0 = dot2(xr[i][2], xr[i][2], q0); q1 = dot2(xr[i][3], xr[i][3], q1); };
#pragma unroll
    for (int T = 0; T < 6; ++T) {
      if (T == 0) wait_vm<17>(); else if (T == 1) wait_vm<14>(); else if (T == 2) wait_vm<11>(); else if (T == 3) wait_vm<8>(); else if (T == 4) wait_vm<5>(); else wait_vm<2>();
      if (T == 0) STAMP(22);
#pragma unroll
      for (int i = 3 * T; i < 3 * T + 3; ++i) {
        pin(xr[i]);
        s0 = dot2(xr[i][0], 0x3f803f80u, s0); s1 = dot2(xr[i][1], 0x3f803f80u, s1); s0 = dot2(xr[i][2], 0x3f803f80u, s0); s1 = dot2(xr[i][3], 0x3f803f80u, s1);
      }
      fence_sched();
    }
    STAMP(1);
    const float mean = quad_sum(s0 + s1) * (1.f / 576.f), nmean = -rbf(mean);
    auto centre = [&](int i) {
      float d0, d1, d2, d3, d4, d5, d6, d7; u32 o0, o1, o2, o3;
      asm volatile("s_nop 1\n\tv_dot2_f32_bf16 %4, %12, %17, %16\n\tv_dot2_f32_bf16 %5, %12, %18, %16\n\tv_dot2_f32_bf16 %6, %13, %17, %16\n\tv_dot2_f32_bf16 %7, %13, %18, %16\n\t"
                   "v_dot2_f32_bf16 %8, %14, %17, %16\n\tv_dot2_f32_bf16 %9, %14, %18, %16\n\tv_dot2_f32_bf16 %10, %15, %17, %16\n\tv_dot2_f32_bf16 %11, %15, %18, %16\n\t"
                   "v_cvt_pk_bf16_f32 %0, %4, %5\n\tv_cvt_pk_bf16_f32 %1, %6, %7\n\tv_cvt_pk_bf16_f32 %2, %8, %9\n\tv_cvt_pk_bf16_f32 %3, %10, %11"
                   : "=v"(o0), "=v"(o1), "=v"(o2), "=v"(o3), "=&v"(d0), "=&v"(d1), "=&v"(d2), "=&v"(d3), "=&v"(d4), "=&v"(d5), "=&v"(d6), "=&v"(d7)
                   : "v"(xr[i][0]), "v"(xr[i][1]), "v"(xr[i][2]), "v"(xr[i][3]), "v"(nmean), "s"(0x00003f80u), "s"(0x3f800000u));
      xr[i] = u32x4{o0, o1, o2, o3};
    };
#pragma unroll
    for (int i = 0; i < 9; ++i) {
      centre(i); lds_w16(xwa + i * 2048, xr[i]);
      if (i % 3 == 0) DMA_A(0, dl + (i / 3) * SLOT_A); else if (i % 3 == 1) DMA_A(1, dl + (i / 3) * SLOT_A); else DMA_A(2, dl + (i / 3) * SLOT_A);
    }
    so += 128 - 72 * i8;
    wait_lgkm0(); bar();
    STAMP(23);
#pragma unroll
    for (int i = 0; i < 9; ++i) {
      xf[2 * i] = lds_r16(xra + 2 * i * 1024); xf[2 * i + 1] = lds_r16(xrb + (2 * i + 1) * 1024);
      centre(9 + i);
      fence_sched();
    }
    touch(sA, tv0, LB + L_DUMP); touch(sW1, tv1, LB + L_DUMP);
    wait_lgkm0(); bar();
    STAMP(24);
#pragma unroll
    for (int i = 0; i < 9; ++i) { lds_w16(xwa + i * 2048, xr[9 + i]); sq(i); sq(9 + i); fence_sched(); }
    {
      const float dm = mean + nmean;
      const float var = __builtin_fmaxf(quad_sum(q0 + q1) * (1.f / 576.f) - dm * dm, 0.f);
      const float rsd = __builtin_amdgcn_rcpf(rbf(__builtin_amdgcn_sqrtf(rbf(rbf(var) + 1e-5f))));
      if (p4 == 0) *(LAS float*)(LB + L_RS + (32 * r + 16 * c + (l >> 2)) * 4) = rsd;
      wait_vm<5>();
      pin(ep); pin(braw);
      if (tid < 256) *(LAS f32x2*)(LB + L_BIAS + tid * 8) = f32x2{blo(braw), bhi(braw)};
      tag = (((u64)(u32)__builtin_amdgcn_readfirstlane(ep[1]) << 32) | (u32)__builtin_amdgcn_readfirstlane(ep[0])) + 1;
    }
    wait_lgkm0(); bar();
#pragma unroll
    for (int i = 0; i < 9; ++i) { xf[18 + 2 * i] = lds_r16(xra + 2 * i * 1024); xf[19 + 2 * i] = lds_r16(xrb + (2 * i + 1) * 1024); }
    rs = *(LAS float*)(LB + L_RS + (32 * r + (l & 31)) * 4);
  }
  STAMP(2);

  const u32x4 sG = make_srd(ws + WS_G + (kq * 16 + m) * G_SZ, G_SZ);
  f32x16 prev = {};
  u32 ba = LB + L_BIAS + c * 128 + h * 16;
  u32 hso = (8 * s + c) * 8192, gvo;
  {
    const u32 wl = LB + h * 1024 + q4 * 128 + 64 * (c ^ ((q4 >> 1) & 1)) + 32 * blk + 8 * p4;
    constexpr int PF = 4;
    bf16x8 wf[PF];
#pragma unroll
    for (int j = 0; j < PF; ++j) wf[j] = tr_frag(wl + j * 2048, wl + j * 2048 + 512);
    u32 R0 = 0, R1 = SLOT_A, R2 = 2 * SLOT_A, R3 = 3 * SLOT_A;
    f32x16 acc;
    auto kstep = [&](auto KK) __attribute__((always_inline)) {
      constexpr int kk = decltype(KK)::v, sl = kk / 12, ks = kk % 12;
      if constexpr (kk == 0) acc = MFMA32(wf[0], as_bf(xf[0]), f32x16{});
      else if constexpr (kk < 35) acc = MFMA32(wf[ks % PF], as_bf(xf[kk]), acc);
      else asm volatile("v_mfma_f32_32x32x16_bf16 %0, %1, %2, %3" : "=&v"(prev) : "v"(wf[ks % PF]), "v"(xf[kk]), "v"(acc));
      if constexpr (kk < 35) pin(acc);
      fence_sched();
      constexpr int sn = (kk + PF) / 12;
      const u32 pa = wl + (sn == 0 ? R0 : sn == 1 ? R1 : sn == 2 ? R2 : R3) + ((ks + PF) % 12) * 2048;
      wf[ks % PF] = tr_frag(pa, pa + 512);
      fence_sched();
      const u32 dst = dl + (sl == 0 ? R3 : sl == 1 ? R0 : R1);
      if constexpr (ks == 1) DMA_A(0, dst);
      if constexpr (ks == 5) DMA_A(1, dst);
      if constexpr (ks == 9) DMA_A(2, dst);
    };
    wait_lgkm0(); bar();
    STAMP(3);
    {
      const u32 qvo = h * 1024 + (32 * r + (l & 31)) * 8;
      const u32x4 sQ = make_srd(ws + WS_QKV + (kq * 16 + m) * QKV_SZ + s * 36864 - 32768, isq ? 36864 + 32768 : 0);
      int oq = (32 * c - 16 * kq - 64) * 256;
      const u32x4 sW2 = make_srd(w2 + 1152 * kq * 288, 1152 * 1152);
      u32 tv2 = (isq && l < 14) ? ((((m & 7) * 3 + s) * 8 + w) * 56 + l) * 128 : OOB;
      const float re = s == 0 ? rs * 0.34004557f : rs;
#pragma unroll 1
      for (int ci = 0; ci <= NQ; ++ci) {
        u32x2 un;
        sfor<0, 36>([&](auto KK) __attribute__((always_inline)) {
          constexpr int kk = decltype(KK)::v;
          kstep(KK);
          if constexpr (kk == 2 || kk == 3 || kk == 6 || kk == 7 || kk == 14 || kk == 15 || kk == 18 || kk == 19) {
            constexpr int e = (kk & 1) + ((kk >> 2) & 1) * 2 + (kk >= 14 ? 4 : 0) - (kk >= 14 ? (((kk >> 2) & 1) * 2 - (kk >= 18 ? 2 : 0)) : 0);
            u32 v = pk2(prev[2 * e] * re, prev[2 * e + 1] * re); pin(v); un[e & 1] = v;
          }
          if constexpr (kk == 4 || kk == 10 || kk == 16 || kk == 22) {
            const int o = kk < 12 ? oq : oq + 4096;
            if constexpr (kk == 4 || kk == 16) st8c<0>(un, sQ, qvo, o < 0 ? OOB : (u32)o + 32768); else st8c<2048>(un, sQ, qvo, o < 0 ? OOB : (u32)o + 32768);
          }
          if constexpr (kk == 34) {
            if (RARE(ci == 0)) {
              int b_ = bid, t_ = tid; pins(b_); pin(t_);
              u32x4 sv = {SENT, SENT, SENT, SENT}; pin(sv);
#pragma unroll
              for (int i = 0; i < 4; ++i) st16c<0>(sv, sWS, (i < 3 || t_ < 192) ? t_ * 16 + i * 8192 : OOB, WS_P + b_ * 27648);
            }
          }
          if constexpr (kk == 34) {
            touch(sW2, tv2, LB + L_DUMP); tv2 += 14 * 128;
            oq += 16384;
            so += 128 - 72 * i8;
            if (RARE(ci + 2 == NQ)) { sA = sW1; voA = vo_f; i8 = 8 * 9216; i176 = 176 * 9216; so = 24 * w * 9216 + fcol - 176 * 9216; }
          }
          fence_sched();
          if constexpr (kk == 35) { const u32 t = R3; R3 = R2; R2 = R1; R1 = R0; R0 = t; }
          if constexpr (kk % 12 == 11) { if constexpr (kk == 11) wait_vm<6>(); else if constexpr (kk == 23) wait_vm<6>(); else wait_vm<5>(); bar(); }
        });
      }
    }
    STAMP(4);
    gvo = 2 * r * 1024 + blk * 1024 + h * 256 + t16 * 16;
    f32x4 bq = *(LAS f32x4*)ba;
#pragma unroll 1
    for (int ci = NQ + 1;; ++ci) {
      u32x4 un[2];
      float xa, xb, ea, eb;
      const bool ev = ci == 4 || ci == NC - 1;
      sfor<0, 36>([&](auto KK) __attribute__((always_inline)) {
        constexpr int kk = decltype(KK)::v;
        kstep(KK);
        if constexpr (kk >= 2 && kk < 34) {
          constexpr int t = kk - 2, e = 2 * (t >> 2);
          if constexpr ((t & 3) == 0) { xa = __builtin_fmaf(prev[e], rs, bq[e & 3]); ea = gelu_a(xa); pin(ea); }
          if constexpr ((t & 3) == 1) { xb = __builtin_fmaf(prev[e + 1], rs, bq[(e & 3) + 1]); eb = gelu_a(xb); pin(eb); }
          if constexpr ((t & 7) == 5) { if constexpr (t == 29) ba += 256; bq = *(LAS f32x4*)(ba + (((t >> 3) + 1) & 3) * 32); }
          if constexpr ((t & 3) == 2) { ea = gelu_b(xa, ea); pin(ea); }
          if constexpr ((t & 3) == 3) { u32 v = pk2(ea, gelu_b(xb, eb)); pin(v); un[t >> 4][(t >> 2) & 3] = v; }
        }
        if constexpr (kk == 0) {
          if (RARE(ev)) {
            if (ci == 4) wait_vm<0>();
            else {
              int m_ = m, w_ = w, t_ = tid; pins(m_); pins(w_); pin(t_);
              const int l_ = (t_ >> 1) & 7, pm = l_ < 3 ? m_ : (l_ < 5 ? m_ - 1 : m_ - 2);
              asm volatile("s_mov_b32 m0, %0\n\ts_mov_b64 exec, 0xffff\n\tbuffer_load_dword %1, %2, 0 offen sc1 lds\n\ts_mov_b64 exec, -1"
                           :: "s"(LB + L_FLG + w_ * 64), "v"(WS_FLAG + bid_of(pm < 0 ? m_ : pm, kq, l_ < 3 ? l_ : 1 + (l_ & 1)) * 128 + (t_ & 1) * 4), "s"(sWS) : "memory");
            }
          }
        }
        if constexpr (kk == 12) {
          if (RARE(ev)) {
            int m_ = m, w_ = w, b_ = bid, t_ = tid; pins(m_); pins(w_); pins(b_); pin(t_);
            if (ci == 4) { if (t_ == 0) __hip_atomic_store((GAS u64*)(ws + WS_FLAG + b_ * 128), tag, RLX_AGENT); }
            else {
            wait_vm<3>();
            const u32x2 fl = lds_r8(LB + L_FLG + w_ * 64 + (t_ & 7) * 8);
            const int l_ = t_ & 7, pm = l_ < 3 ? m_ : (l_ < 5 ? m_ - 1 : m_ - 2);
            if (!all_lanes(fl[0] == (u32)tag && fl[1] == (u32)(tag >> 32))) poll_words(ws, WS_FLAG + bid_of(pm < 0 ? m_ : pm, kq, l_ < 3 ? l_ : 1 + (l_ & 1)) * 128, true, tag);
            const u32 src = WS_QKV + (kq * 16 + m_ - 2) * QKV_SZ + (36 + w_) * 1024 + 9216 * s, dd = LB + L_KV + w_ * PP, lv = (t_ & 63) * 16;
            const u32 s0 = m_ >= 2 ? src : OOB, s1 = m_ >= 1 ? src + QKV_SZ : OOB, s2 = src + 2 * QKV_SZ;
            dma16c<0, 0>(sWS, lv, s0, dd); dma16c<9 * PP, 36864>(sWS, lv, s0, dd);
            dma16c<1024, 0>(sWS, lv, s1, dd); dma16c<9 * PP + 1024, 36864>(sWS, lv, s1, dd);
            dma16c<2048, 0>(sWS, lv, s2, dd); dma16c<9 * PP + 2048, 36864>(sWS, lv, s2, dd);
            if (w_ < 6) {
              const int mt = w_ >> 1, kv = w_ & 1;
              dma16c<0, 0>(sWS, lv, (mt == 0 ? s0 : mt == 1 ? s1 : s2) + (8 - w_) * 1024 + kv * 36864, LB + L_KV + (8 + 9 * kv) * PP + mt * 1024);
              *(LAS u32x2*)(LB + L_KV + 18 * PP + t_ * 8) = u32x2{t_ < 256 - 128 * m_ ? 0xff7f3f80u : 0x00003f80u, 0x3f803f80u};
            }
            }
          }
        }
        if constexpr (kk == 22) st16c<0>(un[0], sG, gvo, hso);
        if constexpr (kk == 34) {
          st16c<512>(un[1], sG, gvo, hso);
          hso += 16384;
          so += 128 - 576 * 9216;
          if (RARE(ci + 2 == NC)) { sA[2] = 0; dl = L_TOTAL; }
        }
        fence_sched();
        if constexpr (kk == 35) { const u32 t = R3; R3 = R2; R2 = R1; R1 = R0; R0 = t; }
        if constexpr (kk == 11) { wait_vm<4>(); bar(); }
        if constexpr (kk == 23) { if (ci != NC - 1) { wait_vm<4>(); bar(); } }
      });
      if (ci == NC - 1) break;
      wait_vm<5>(); bar();
    }
  }
  STAMP(10);
  const auto rWS = __builtin_amdgcn_make_buffer_rsrc(ws, 0, WS_END, 0x00020000);
  const auto rG = __builtin_amdgcn_make_buffer_rsrc(ws + WS_G + (kq * 16 + m) * G_SZ, 0, G_SZ, 0x00020000);
  const u32x4 sB = make_srd(w2 + 1152 * kq * 288, 1152 * 1152);
  u32x4 qf, qt = {};
  {
    const u32 qo = WS_QKV + (kq * 16 + m) * QKV_SZ + 9216 * s, qv = (32 * r + (l & 31)) * 8;
    qf = cat(__builtin_amdgcn_raw_buffer_load_b64(rWS, qv + (5 * c + 2 * h) * 1024, qo, 16), __builtin_amdgcn_raw_buffer_load_b64(rWS, qv + (5 * c + 2 * h + 1) * 1024, qo, 16));
    const u32 qq = __builtin_amdgcn_raw_buffer_load_b32(rWS, h ? OOB : qv + 4096 + 4 * c, qo, 16);
    qt[0] = c ? 0u : qq; qt[1] = c ? qq : 0u; qt[2] = h ? 0u : 0x3f800000u;
  }
  u32 LG[4];
#pragma unroll
  for (int i = 0; i < 4; ++i) { const int g = (i + s) & 3; LG[i] = g == 3 ? 14 : 4 * g; }
  const u32 gl0 = c * 8192 + (2 * r + c) * 1024 + l * 16, gl1 = gl0 ^ 1024;
  u32x4 gf[6][2];
#define G_OFF(n) ((n) < 16 ? (LG[((n) >> 2) & 3] + ((n) & 3)) * 16384 : (u32)((n) < 18 ? (n) - 4 : (n)) * 16384)
#define ISSUE_G(n, e) gf[(n) % 6][e] = __builtin_amdgcn_raw_buffer_load_b128(rG, (e) ? gl1 : gl0, G_OFF(n), 16)
  ISSUE_G(0, 0); ISSUE_G(0, 1); ISSUE_G(1, 0); ISSUE_G(1, 1);
  u32 vb[3];
#pragma unroll
  for (int i = 0; i < 3; ++i) { const int pc = 64 * (w + 8 * i) + l, pp = pc % 18; vb[i] = (pc / 18) * 1152 + (pp < 16 ? pp ^ (4 * kq) : pp) * 16 + 288 * s; }
  const u64 m3 = __builtin_amdgcn_ballot_w64(tid < 128);
  const u32 db = LB + w * 1024;
  u32 WG[4];
#pragma unroll
  for (int i = 0; i < 4; ++i) WG[i] = LG[i] * 73728;
  auto issue_w = [&](auto N_, auto I_) __attribute__((always_inline)) {
    constexpr int n = decltype(N_)::v, i = decltype(I_)::v, LI = (n % 5) * SLOT_B + i * 8192, SI = (n < 16 ? (n & 3) : n - 4) * 73728;
    const u32 base = n < 16 ? WG[(n >> 2) & 3] : 0u;
    if constexpr (i < 2) dma16<LI, SI>(sB, vb[i], base, db); else dma16m<LI, SI>(sB, vb[2], base, db, m3);
  };
#define ISSUE_W(n, i) issue_w(IC<(n)>{}, IC<(i)>{})
  {
    asm volatile("s_nop 7\n\ts_nop 3" ::: "memory");
    u32x4 un[2];
    sfor<0, 4>([&](auto G_) __attribute__((always_inline)) {
      constexpr int gq = decltype(G_)::v;
      const f32x4 b = *(LAS f32x4*)(ba + gq * 32);
      float y[4];
#pragma unroll
      for (int e = 0; e < 4; ++e) { const float xx = __builtin_fmaf(prev[4 * gq + e], rs, b[e]); y[e] = gelu_b(xx, gelu_a(xx)); }
      un[gq >> 1][2 * (gq & 1)] = pk2(y[0], y[1]); un[gq >> 1][2 * (gq & 1) + 1] = pk2(y[2], y[3]);
      if constexpr (gq & 1) pin(un[gq >> 1]);
    });
    pin(qf); pin(qt);
    wait_lgkm0(); bar();
    st16c<0>(un[0], sG, gvo, hso); st16c<512>(un[1], sG, gvo, hso);
    ISSUE_G(2, 0); ISSUE_G(2, 1);
#pragma unroll
    for (int e = 0; e < 4; ++e) {
      auto a = __builtin_amdgcn_permlane32_swap(un[0][e], un[1][e], false, false);
      auto b = __builtin_amdgcn_permlane16_swap(a[0], a[1], false, false);
      gf[3][0][e] = c ? b[1] : b[0]; gf[3][1][e] = c ? b[0] : b[1];
    }
  }
  STAMP(11);
  u64 hfl;
  {
    u32 ka = LB + L_KV + (32 * r + (l & 31)) * 8, kb = ka + 4 * PP;
    ka += (5 * c + 2 * h) * PP;
    u32 va = LB + L_KV + (32 * r + 4 * h + q4) * 8 + (blk ? (p4 ? 18 : 13) : 9 + 5 * c + p4) * PP;
    pin(ka); pin(kb); pin(va);
#define K_MAIN(i) as_bf(cat(lds_r8(ka + (i) * 256), lds_r8(ka + PP + (i) * 256)))
#define K_TAIL(i) as_bf(cat(lds_r8(kb + (i) * 256), lds_r8(kb + 14 * PP + (i) * 256)))
    bf16x8 km[3], kt[3], k8;
    f32x16 st[9];
    km[0] = K_MAIN(0); kt[0] = K_TAIL(0); km[1] = K_MAIN(8); kt[1] = K_TAIL(8); k8 = kt[1];
    float mx0, mx1;
    sfor<0, 11>([&](auto I_) __attribute__((always_inline)) {
      constexpr int ii = decltype(I_)::v;
      if constexpr (ii + 2 < 9) { km[(ii + 2) % 3] = K_MAIN(ii + 1); kt[(ii + 2) % 3] = K_TAIL(ii + 1); }
      if constexpr (ii < 9) {
        st[ii] = MFMA32(km[ii % 3], as_bf(qf), f32x16{});
        st[ii] = MFMA32(kt[ii % 3], as_bf(qt), st[ii]);
        pin(st[ii]);
      }
      if constexpr (ii < 6) ISSUE_W(ii / 3, ii % 3);
      if constexpr (ii == 3) {
#pragma unroll
        for (int e = 0; e < 16; ++e) st[1][e] = __builtin_amdgcn_inverse_ballot_w64(diag_mask(e)) ? st[1][e] : st[0][e];
        mx0 = st[1][0]; mx1 = st[1][1];
      }
      if constexpr (ii >= 3) {
        constexpr int i = ii - 2;
#pragma unroll
        for (int e = (ii == 3 ? 2 : 0); e < 16; e += 2) { if (e & 2) mx0 = max3(mx0, st[i][e], st[i][e + 1]); else mx1 = max3(mx1, st[i][e], st[i][e + 1]); }
        pin(mx0); pin(mx1);
      }
      fence_sched();
    });
    float mx;
    { auto sw = __builtin_amdgcn_permlane32_swap(f2u(mx0), f2u(mx1), false, false); mx = max3(u2f(sw[0]), u2f(sw[1]), u2f(sw[0]));
      auto s2 = __builtin_amdgcn_permlane32_swap(f2u(mx), f2u(mx), false, false); mx = max3(u2f(s2[0]), u2f(s2[1]), u2f(s2[0])); }
    u32x4 qm = {};
    { const float n0 = -mx, a0 = bhi(f2u(n0)), n1 = n0 - a0, a1 = bhi(f2u(n1)), n2 = n1 - a1;
      qm[2] = h ? 0u : f2u(a0) >> 16; qm[3] = h ? 0u : ((f2u(a1) >> 16) | (f2u(n2) & 0xffff0000u)); }
    st[1] = MFMA32(k8, as_bf(qm), st[1]); st[2] = MFMA32(k8, as_bf(qm), st[2]); pin(st[1]); pin(st[2]);
    bf16x8 vf[2], v0[2];
    vf[0] = tr_frag(va + 8 * 256, va + 8 * 256 + 64);
    v0[0] = tr_frag(va, va + 64); v0[1] = tr_frag(va + 128, va + 192);
    const u32x4 mk0 = lds_r16(LB + L_MSK + l * 16), mk1 = lds_r16(LB + L_MSK + 1024 + l * 16);
    STAMP(12);
    fence_sched(); wait_vm<0>(); bar();
    STAMP(13);
    if (tid == 0) __hip_atomic_store((GAS u64*)(ws + WS_FLAG + (256 + bid) * 128), tag, RLX_AGENT);
    f32x16 o;
    sfor<0, 16>([&](auto J_) __attribute__((always_inline)) {
      constexpr int j = decltype(J_)::v, i = 1 + (j >> 1), u = j & 1, jn = j + 1, tn = (jn >> 1) == 0 ? 8 : (jn >> 1);
      if constexpr (jn < 16) vf[jn & 1] = tr_frag(va + tn * 256 + (jn & 1) * 128, va + tn * 256 + (jn & 1) * 128 + 64);
      if constexpr (u == 0 && i + 2 < 9) { st[i + 2] = MFMA32(k8, as_bf(qm), st[i + 2]); pin(st[i + 2]); }
      u32x4 pf;
#pragma unroll
      for (int e = 0; e < 4; ++e) pf[e] = pk2(__builtin_amdgcn_exp2f(st[i][8 * u + 2 * e]), __builtin_amdgcn_exp2f(st[i][8 * u + 2 * e + 1]));
      if constexpr (i == 1) {
        const u32x4 mk = u ? mk1 : mk0;
        if constexpr (j == 0) o = MFMA32(v0[u], as_bf(pf & ~mk), f32x16{}); else o = MFMA32(v0[u], as_bf(pf & ~mk), o);
        pf &= mk;
      }
      o = MFMA32(vf[u], as_bf(pf), o); pin(o);
      if constexpr (j >= 1 && j < 10) ISSUE_W(2 + (j - 1) / 3, (j - 1) % 3);
      if constexpr (j == 12) hfl = __builtin_bit_cast(u64, __builtin_amdgcn_raw_buffer_load_b64(rWS, (l & 3) * 1024, WS_FLAG + (256 + (bid & ~24)) * 128, 16));
      fence_sched();
    });
    const float inv = __builtin_amdgcn_rcpf(h ? o[8] : o[12]);
    if (RARE(!all_lanes(hfl == tag))) poll_words(ws, WS_FLAG + (256 + (bid & ~24)) * 128 + (l & 3) * 1024, true, tag);
    ISSUE_G(4, 0); ISSUE_G(4, 1);
    st16c<0>(u32x4{pk2(o[0] * inv, o[1] * inv), pk2(o[2] * inv, o[3] * inv), pk2(o[4] * inv, o[5] * inv), pk2(o[6] * inv, o[7] * inv)}, sG, gvo, (36 + s) * 8192 + c * 512);
    st16c<0>(u32x4{c ? pk2(o[10] * inv, o[11] * inv) : pk2(o[8] * inv, o[9] * inv), 0u, 0u, 0u}, sG, h ? OOB : gvo, (40 + (s >> 1)) * 8192 + (2 * (s & 1) + c) * 256);
  }
  STAMP(14);
  f32x4 ac[2][9];
  {
    u32 wb = LB + c * 9216 + (16 * (g4 >> 1) + 4 * (g4 & 1) + q4) * 288 + 8 * p4, wb2 = wb + 3 * SLOT_B, wo1, wo2;
    pin(wb); pin(wb2);
    bf16x8 af[9];
#pragma unroll
    for (int d = 0; d < 8; ++d) af[d] = tr_frag(wb + d * 32, wb + d * 32 + 2304);
    STAMP(15);
    sfor<0, 189>([&](auto Q_) __attribute__((always_inline)) {
      constexpr int q = decltype(Q_)::v, b = q / 9, d = q % 9;
      constexpr int qn = q + 8, bn = qn / 9, dn = qn % 9, rb = bn % 5;
      const u32 pa = (bn < 18 ? (rb < 3 ? wb + rb * SLOT_B : wb2 + (rb - 3) * SLOT_B) : bn < 20 ? wo1 + (bn - 18) * 72 * 288 : wo2) + dn * 32;
      s16x4 lo, hi;
      if constexpr (b == 0) ac[0][d] = MFMA16(af[d], as_bf(gf[b % 6][0]), f32x4{}); else ac[0][d] = MFMA16(af[d], as_bf(gf[b % 6][0]), ac[0][d]);
      pin(ac[0][d]); fence_sched();
      if constexpr (bn < 21) lo = __builtin_amdgcn_ds_read_tr16_b64_v4i16((LAS s16x4*)pa);
      fence_sched();
      if constexpr (b == 0) ac[1][d] = MFMA16(af[d], as_bf(gf[b % 6][1]), f32x4{}); else ac[1][d] = MFMA16(af[d], as_bf(gf[b % 6][1]), ac[1][d]);
      pin(ac[1][d]); fence_sched();
      if constexpr (bn < 21) { hi = __builtin_amdgcn_ds_read_tr16_b64_v4i16((LAS s16x4*)(pa + 2304)); af[dn] = __builtin_bit_cast(bf16x8, __builtin_shufflevector(lo, hi, 0, 1, 2, 3, 4, 5, 6, 7)); }
      fence_sched();
      if constexpr (b >= 1 && b + 4 < 18) { if constexpr (d == 1) ISSUE_W(b + 4, 0); if constexpr (d == 4) ISSUE_W(b + 4, 1); if constexpr (d == 6) ISSUE_W(b + 4, 2); }
      if constexpr (b + 5 < 21) { if constexpr (d == 2) ISSUE_G(b + 5, 0); if constexpr (d == 7) ISSUE_G(b + 5, 1); }
      if constexpr (q == 27) wait_vm<0>();
      if constexpr (q == 36) { if (tid == 0) __hip_atomic_store((GAS u64*)(ws + WS_FLAG + (512 + bid) * 128), tag, RLX_AGENT); }
      if constexpr (q == 90) hfl = __builtin_bit_cast(u64, __builtin_amdgcn_raw_buffer_load_b64(rWS, (l & 4) ? ((bid & ~3) + (l & 3)) * 128 : (512 + (bid & ~24)) * 128 + (l & 3) * 1024, WS_FLAG, 16));
      if constexpr (q == 117) {
        if (RARE(!all_lanes(hfl == tag))) poll_words(ws, WS_FLAG + ((l & 4) ? ((bid & ~3) + (l & 3)) * 128 : (512 + (bid & ~24)) * 128 + (l & 3) * 1024), true, tag);
      }
      if constexpr (b == 14 || b == 15) {
        const u32x4 sWO = make_srd(wo + 144 * kq * 288, 144 * 1152);
        if constexpr (d == 0) dma16<L_KV + (b - 14) * SLOT_B, (b - 14) * 73728>(sWO, vb[0], 0u, db);
        if constexpr (d == 3) dma16<L_KV + (b - 14) * SLOT_B + 8192, (b - 14) * 73728>(sWO, vb[1], 0u, db);
        if constexpr (d == 5) dma16m<L_KV + (b - 14) * SLOT_B + 16384, (b - 14) * 73728>(sWO, vb[2], 0u, db, m3);
        if constexpr (b == 15 && d == 8) { if (w < 6) dma16<L_KV + 2 * SLOT_B, 2 * 73728>(sWO, vb[0], 0u, db); }
      }
      if constexpr (q == 145) {
        wo1 = LB + L_KV + (36 * c + 20 * (g4 >> 1) + 4 * (g4 & 1) + q4) * 288 + 8 * p4;
        wo2 = LB + L_KV + (72 * c + 36 * (g4 >> 1) + 2 * (g4 & 1) + 16 + q4) * 288 + 8 * p4;
        pin(wo1); pin(wo2);
      }
      if constexpr (q == 155) {
        const u32x4 sX = make_srd(x, 2048 * 1152);
        dma16<L_RES, 0>(sX, (l >> 2) * 1152 + p4 * 16 + 288 * s + 64 * kq, xso, LB + (2 * r + c) * 1024);
        if (w == 0) dma16<L_RES + 8192, 0>(sX, (l >> 1) * 1152 + (l & 1) * 16 + 288 * s + 256, (128 * m + 32 * kq) * 1152, LB);
        if (w == 1) dma16<L_RES + 9216, 0>(make_srd(b2, 1152), l * 16 + 288 * s, 0, LB);
        if (bid == 24 && w == 4) {
#pragma unroll 1
          for (int i = 0; i < 4; ++i) poll_words(ws, WS_FLAG + (l + 64 * i) * 128, true, tag);
          if (l == 0) __hip_atomic_store((GAS u64*)(ws + WS_EPOCH), tag, RLX_AGENT);
        }
      }
      if constexpr (b == 20 && d >= 1) *(LAS f32x4*)(LB + w * 9216 + l * 16 + (d - 1) * 1024) = ac[1][d - 1];
      fence_sched();
      if constexpr (d == 8 && b < 19) {
        if constexpr (b == 0) wait_vm<6>(); else if constexpr (b >= 3 && b <= 15) wait_vm<8>(); else if constexpr (b == 16) wait_vm<0>();
        if constexpr (b != 17) bar();
        STAMP(41 + b);
      }
    });
  }
  STAMP(16);
  {
    *(LAS f32x4*)(LB + w * 9216 + l * 16 + 8 * 1024) = ac[1][8];
    wait_vm<0>(); wait_lgkm0(); bar();
    f32x4 v[9];
#pragma unroll
    for (int d = 8; d >= 0; --d) v[d] = ac[0][d] + *(LAS f32x4*)(LB + (w ^ 4) * 9216 + l * 16 + d * 1024);
#pragma unroll
    for (int k = 3; k >= 1; --k)
      st16c<0>(u32x4{umin(pk2(v[2 * k][0], v[2 * k][1]), SENT - 1), umin(pk2(v[2 * k][2], v[2 * k][3]), SENT - 1), umin(pk2(v[2 * k + 1][0], v[2 * k + 1][1]), SENT - 1), umin(pk2(v[2 * k + 1][2], v[2 * k + 1][3]), SENT - 1)},
               sWS, l * 16, WS_P + (bid ^ k) * 27648 + (k - 1) * 9216 + w * 1024);
    const bool own8 = r == kq;
    st8c<0>(u32x2{umin(pk2(v[8][0], v[8][1]), SENT - 1), umin(pk2(v[8][2], v[8][3]), SENT - 1)}, sWS, l * 8, own8 ? OOB : WS_P + (bid ^ kq ^ r) * 27648 + ((kq ^ r) - 1) * 9216 + 8192 + c * 512);
    STAMP(19);
    const u32 ra = LB + L_RES + (2 * r + c) * 1024 + t16 * 64 + g4 * 8, r8 = LB + L_RES + 8192 + (16 * c + t16) * 32 + g4 * 8, rb = LB + L_RES + 9216 + g4 * 8;
    {
      const u32x2 x0 = lds_r8(ra), x1 = lds_r8(ra + 32), x8 = lds_r8(r8), b0 = lds_r8(rb + 64 * kq), b1_ = lds_r8(rb + 64 * kq + 32), b8 = lds_r8(rb + 256);
      v[0] += f32x4{blo(x0[0]), bhi(x0[0]), blo(x0[1]), bhi(x0[1])} + f32x4{blo(b0[0]), bhi(b0[0]), blo(b0[1]), bhi(b0[1])};
      v[1] += f32x4{blo(x1[0]), bhi(x1[0]), blo(x1[1]), bhi(x1[1])} + f32x4{blo(b1_[0]), bhi(b1_[0]), blo(b1_[1]), bhi(b1_[1])};
      v[8] += f32x4{blo(x8[0]), bhi(x8[0]), blo(x8[1]), bhi(x8[1])} + f32x4{blo(b8[0]), bhi(b8[0]), blo(b8[1]), bhi(b8[1])};
      pin(v[0]); pin(v[1]); pin(v[8]);
    }
    const u32x4 sOUT = make_srd(out, 2048 * 1152);
    u32 ov = (l >> 2) * 1152 + p4 * 16 + 288 * s + 64 * kq, ov8 = (own8 && l < 32) ? (l >> 1) * 1152 + (l & 1) * 16 + 288 * s + 256 : OOB;
    pin(ov); pin(ov8);
    const u32 po = WS_P + bid * 27648;
    u32x4 pa[3]; u32x2 pb[3];
#pragma unroll 1
    for (u32 spins = 0; spins < SPINS; ++spins) {
      asm volatile("" ::: "memory");
#pragma unroll
      for (int sg = 0; sg < 3; ++sg) {
        pa[sg] = __builtin_amdgcn_raw_buffer_load_b128(rWS, l * 16, po + sg * 9216 + w * 1024, 16);
        pb[sg] = __builtin_amdgcn_raw_buffer_load_b64(rWS, l * 8, own8 ? po + sg * 9216 + 8192 + c * 512 : OOB, 16);
      }
      u32 mxu = 0;
#pragma unroll
      for (int sg = 0; sg < 3; ++sg) mxu = umax(umax(umax(mxu, umax(pa[sg][0], pa[sg][1])), umax(pa[sg][2], pa[sg][3])), umax(pb[sg][0], pb[sg][1]));
      if (all_lanes(mxu != SENT)) break;
      __builtin_amdgcn_s_sleep(1);
    }
    STAMP(20);
#pragma unroll
    for (int sg = 0; sg < 3; ++sg) {
      v[0] += f32x4{blo(pa[sg][0]), bhi(pa[sg][0]), blo(pa[sg][1]), bhi(pa[sg][1])};
      v[1] += f32x4{blo(pa[sg][2]), bhi(pa[sg][2]), blo(pa[sg][3]), bhi(pa[sg][3])};
    }
    *(LAS u32x2*)ra = u32x2{pk2(v[0][0], v[0][1]), pk2(v[0][2], v[0][3])};
    *(LAS u32x2*)(ra + 32) = u32x2{pk2(v[1][0], v[1][1]), pk2(v[1][2], v[1][3])};
    const u32x4 t = lds_r16(LB + L_RES + (2 * r + c) * 1024 + l * 16);
    asm volatile("buffer_store_dwordx4 %0, %1, %2, %3 offen\n\ts_nop 1" :: "v"(t), "v"(ov), "s"(sOUT), "s"(xso) : "memory");
    if (own8) {
#pragma unroll
      for (int sg = 0; sg < 3; ++sg) v[8] += f32x4{blo(pb[sg][0]), bhi(pb[sg][0]), blo(pb[sg][1]), bhi(pb[sg][1])};
      *(LAS u32x2*)r8 = u32x2{pk2(v[8][0], v[8][1]), pk2(v[8][2], v[8][3])};
      const u32x4 t8 = lds_r16(LB + L_RES + 8192 + c * 512 + (l & 31) * 16);
      asm volatile("buffer_store_dwordx4 %0, %1, %2, %3 offen\n\ts_nop 1" :: "v"(t8), "v"(ov8), "s"(sOUT), "s"(xso) : "memory");
    }
  }
}
#if defined(__HIP_DEVICE_COMPILE__)
#pragma clang attribute pop
#endif

extern "C" void kernel_launch(void* const* d_in, const int* in_sizes, int n_in, void* d_out, int out_size, void* d_ws, size_t ws_size, hipStream_t stream) {
  fused_block<<<256, 512, 0, stream>>>((const u32*)d_in[0], (const u32*)d_in[1], (const u32*)d_in[2], (const u32*)d_in[4], (const u32*)d_in[5],
                                       (const u32*)d_in[6], (const u32*)d_in[7], (u32*)d_out, (char*)d_ws);
}
```

```cpp
#define PF_CODE
#define PF_TLB
#define PF_TLB_N 18
#define PF_TLB_STRIDE 65536
#include <hip/hip_runtime.h>
#pragma clang diagnostic ignored "-Wint-to-pointer-cast"
#if defined(__HIP_DEVICE_COMPILE__)
#pragma clang attribute push (__attribute__((target("no-packed-fp32-ops"))), apply_to = function)
#endif
typedef unsigned u32;
typedef unsigned long long u64;
typedef short s16x4 __attribute__((ext_vector_type(4)));
typedef __bf16 bf16x8 __attribute__((ext_vector_type(8)));
typedef __bf16 bf16x2 __attribute__((ext_vector_type(2)));
typedef float f32x2 __attribute__((ext_vector_type(2)));
typedef float f32x4 __attribute__((ext_vector_type(4)));
typedef float f32x16 __attribute__((ext_vector_type(16)));
typedef u32 u32x2 __attribute__((ext_vector_type(2)));
typedef u32 u32x4 __attribute__((ext_vector_type(4)));
#define LAS __attribute__((address_space(3)))
#define GAS __attribute__((address_space(1)))
#define DEV __device__ __forceinline__
#define RLX_AGENT __ATOMIC_RELAXED, __HIP_MEMORY_SCOPE_AGENT

constexpr u32 WS_EPOCH = 0;
constexpr u32 WS_FLAG = 1024;
constexpr u32 QKV_SZ = 3 * 36 * 1024;
constexpr u32 WS_QKV = 131072;
constexpr u32 G_SZ = 42 * 8192;
constexpr u32 WS_G = WS_QKV + 64 * QKV_SZ;
constexpr u32 WS_P = WS_G + 64 * G_SZ;
constexpr u32 WS_END = WS_P + 256 * 27648;
constexpr u32 OOB = 0x7ffffff0u;
constexpr u32 PP = 3104;
constexpr u32 L_KV = 98304, L_BIAS = 157312, L_RS = 159360, L_MSK = 159872, L_DUMP = 161920, L_FLG = 162176, L_TOTAL = 163840;
constexpr u32 SLOT_A = 24576, SLOT_B = 18432, L_RES = L_KV + 43008;
constexpr u32 SENT = 0xffffffffu;

DEV u32x4 make_srd(const void* p, u32 bytes) { u64 a = (u64)p; return u32x4{(u32)a, (u32)(a >> 32) & 0xffffu, bytes, 0x00020000u}; }
template <int LI, int SI> DEV void dma16(u32x4 srd, u32 voff, u32 soff, u32 lds) {
  u32 t;
  asm volatile("s_add_u32 m0, %1, %5\n\ts_add_u32 %0, %4, %6\n\tbuffer_load_dwordx4 %2, %3, %0 offen lds"
               : "=&s"(t) : "s"(lds), "v"(voff), "s"(srd), "s"(soff), "i"(LI), "i"(SI) : "memory", "scc");
}
template <int LI, int SI> DEV void dma16c(u32x4 srd, u32 voff, u32 soff, u32 lds) {
  u32 t;
  asm volatile("s_add_u32 m0, %1, %5\n\ts_add_u32 %0, %4, %6\n\tbuffer_load_dwordx4 %2, %3, %0 offen sc1 lds"
               : "=&s"(t) : "s"(lds), "v"(voff), "s"(srd), "s"(soff), "i"(LI), "i"(SI) : "memory", "scc");
}
template <int LI> DEV void dma16s(u32x4 srd, u32 voff, u32& so, u32 inc, u32 lds) {
  asm volatile("s_add_u32 m0, %1, %5\n\ts_add_u32 %0, %0, %4\n\tbuffer_load_dwordx4 %2, %3, %0 offen lds"
               : "+s"(so) : "s"(lds), "v"(voff), "s"(srd), "s"(inc), "i"(LI) : "memory", "scc");
}
DEV void touch(u32x4 srd, u32 voff, u32 lds) {
  asm volatile("s_mov_b32 m0, %0\n\ts_nop 0\n\tbuffer_load_dword %1, %2, 0 offen lds" :: "s"(lds), "v"(voff), "s"(srd) : "memory");
}
template <int LI, int SI> DEV void dma16m(u32x4 srd, u32 voff, u32 soff, u32 lds, u64 mask) {
  u32 t;
  asm volatile("s_add_u32 m0, %1, %6\n\ts_add_u32 %0, %4, %7\n\ts_mov_b64 exec, %5\n\tbuffer_load_dwordx4 %2, %3, %0 offen lds\n\ts_mov_b64 exec, -1"
               : "=&s"(t) : "s"(lds), "v"(voff), "s"(srd), "s"(soff), "s"(mask), "i"(LI), "i"(SI) : "memory", "scc");
}
template <int OFF> DEV u32x4 ld16(u32x4 srd, u32 voff, u32 soff) {
  u32x4 r; asm volatile("buffer_load_dwordx4 %0, %1, %2, %3 offen offset:%4" : "=v"(r) : "v"(voff), "s"(srd), "s"(soff), "i"(OFF) : "memory"); return r;
}
template <int OFF> DEV void st16c(u32x4 v, u32x4 srd, u32 voff, u32 soff) {
  asm volatile("buffer_store_dwordx4 %0, %1, %2, %3 offen offset:%4 sc1\n\ts_nop 1" :: "v"(v), "v"(voff), "s"(srd), "s"(soff), "i"(OFF) : "memory");
}
template <int OFF> DEV void st8c(u32x2 v, u32x4 srd, u32 voff, u32 soff) {
  asm volatile("buffer_store_dwordx2 %0, %1, %2, %3 offen offset:%4 sc1\n\ts_nop 1" :: "v"(v), "v"(voff), "s"(srd), "s"(soff), "i"(OFF) : "memory");
}
DEV void st4c(u32 v, u32x4 srd, u32 voff, u32 soff) {
  asm volatile("buffer_store_dword %0, %1, %2, %3 offen sc1\n\ts_nop 1" :: "v"(v), "v"(voff), "s"(srd), "s"(soff) : "memory");
}
template <int OFF> DEV void st8(u32x2 v, u32x4 srd, u32 voff, u32 soff) {
  asm volatile("buffer_store_dwordx2 %0, %1, %2, %3 offen offset:%4\n\ts_nop 1" :: "v"(v), "v"(voff), "s"(srd), "s"(soff), "i"(OFF) : "memory");
}
template <class T> DEV void pins(T& v) { asm volatile("" : "+s"(v)); }
template <class T> DEV void pin(T& v) { asm volatile("" : "+v"(v)); }
#define STAMP(i)
#define STAMPR(i)
template <int N> DEV void wait_vm() { asm volatile("s_waitcnt vmcnt(%0)" :: "i"(N) : "memory"); }
DEV void wait_lgkm0() { asm volatile("s_waitcnt lgkmcnt(0)" ::: "memory"); }
DEV void fence_sched() { __builtin_amdgcn_sched_barrier(0); }
DEV void bar() { fence_sched(); __builtin_amdgcn_s_barrier(); fence_sched(); }

DEV float u2f(u32 v) { return __builtin_bit_cast(float, v); }
DEV u32 f2u(float v) { return __builtin_bit_cast(u32, v); }
DEV bf16x8 as_bf(u32x4 v) { return __builtin_bit_cast(bf16x8, v); }
DEV u32 pk2(float a, float b) { return __builtin_bit_cast(u32, __builtin_convertvector(f32x2{a, b}, bf16x2)); }
DEV float blo(u32 d) { return u2f(d << 16); }
DEV float bhi(u32 d) { return u2f(d & 0xffff0000u); }
DEV float rbf(float f) { return blo(pk2(f, f)); }
DEV float max3(float a, float b, float c) { return __builtin_elementwise_maximum(__builtin_elementwise_maximum(a, b), c); }
DEV u32 umax(u32 a, u32 b) { return a > b ? a : b; }
DEV u32 umin(u32 a, u32 b) { return a < b ? a : b; }
DEV bool all_lanes(bool p) { return __builtin_amdgcn_ballot_w64(!p) == 0; }
DEV float dot2(u32 a, u32 b, float c) { return __builtin_amdgcn_fdot2_f32_bf16(__builtin_bit_cast(bf16x2, a), __builtin_bit_cast(bf16x2, b), c, false); }
DEV float quad_sum(float v) {
  v += u2f(__builtin_amdgcn_mov_dpp(f2u(v), 0xb1, 0xf, 0xf, true));
  return v + u2f(__builtin_amdgcn_mov_dpp(f2u(v), 0x4e, 0xf, 0xf, true));
}
DEV u32x2 lds_r8(u32 a) { return *(volatile LAS u32x2*)a; }
DEV u32x4 cat(u32x2 a, u32x2 b) { return u32x4{a[0], a[1], b[0], b[1]}; }
DEV u32x4 lds_r16(u32 a) { return *(LAS u32x4*)a; }
DEV void lds_w16(u32 a, u32x4 v) { *(LAS u32x4*)a = v; }
DEV bf16x8 tr_frag(u32 a0, u32 a1) {
  s16x4 lo = __builtin_amdgcn_ds_read_tr16_b64_v4i16((LAS s16x4*)a0);
  s16x4 hi = __builtin_amdgcn_ds_read_tr16_b64_v4i16((LAS s16x4*)a1);
  return __builtin_bit_cast(bf16x8, __builtin_shufflevector(lo, hi, 0, 1, 2, 3, 4, 5, 6, 7));
}
template <int I> struct IC { static constexpr int v = I; };
template <int I, int N, class F> DEV void sfor(F&& f) { if constexpr (I < N) { f(IC<I>{}); sfor<I + 1, N>(f); } }
#define SPINS (1u << 18)
constexpr int NPF = 1;
#define RARE(x) __builtin_expect(!!(x), 0)
#define MFMA32(a, b, c) __builtin_amdgcn_mfma_f32_32x32x16_bf16((a), (b), (c), 0, 0, 0)
#define MFMA16(a, b, c) __builtin_amdgcn_mfma_f32_16x16x32_bf16((a), (b), (c), 0, 0, 0)

DEV float gelu_a(float x) { return __builtin_amdgcn_exp2f(x * __builtin_fmaf(x * x, -0.10294324f, -2.3022082f)); }
DEV float gelu_b(float x, float e) { return x * __builtin_amdgcn_rcpf(1.f + e); }
DEV void poll_words(const char* ws, u32 off, bool active, u64 tag) {
#pragma unroll 1
  for (u32 spins = 0; spins < SPINS; ++spins) {
    u64 v = active ? __hip_atomic_load((GAS u64*)(ws + off), RLX_AGENT) : tag;
    if (all_lanes(v == tag)) break;
    __builtin_amdgcn_s_sleep(2);
  }
  __builtin_amdgcn_fence(__ATOMIC_ACQUIRE, "wavefront");
}
DEV int bid_of(int m, int kq, int s) { return ((m & 7) * 4 + s) * 8 + (m >> 3) * 4 + kq; }
constexpr u64 diag_mask(int e) {
  u64 mk = 0;
  for (int l = 0; l < 64; ++l) if ((e & 3) + 8 * (e >> 2) + 4 * (l >> 5) <= (l & 31)) mk |= 1ull << l;
  return mk;
}

__global__ __launch_bounds__(512, 2) void fused_block(const u32* __restrict__ x, const u32* __restrict__ wqkv, const u32* __restrict__ wo,
                                                      const u32* __restrict__ w1, const u32* __restrict__ b1, const u32* __restrict__ w2,
                                                      const u32* __restrict__ b2, u32* __restrict__ out, char* __restrict__ ws) {
  __shared__ __attribute__((aligned(1024))) char smem[L_TOTAL];
  const u32 LB = (u32)(size_t)(LAS char*)smem;
  const int tid = __builtin_amdgcn_workitem_id_x(), l = tid & 63;
  const int w = __builtin_amdgcn_readfirstlane(tid >> 6) & 7, r = w & 3, c = w >> 2;
  const int bid = __builtin_amdgcn_workgroup_id_x(), kq = bid & 3, mh = (bid >> 2) & 1, m = 8 * mh + (bid >> 5), s = (bid >> 3) & 3;
  STAMPR(62); STAMP(0);
  const int h = l >> 5, t16 = l & 15, g4 = l >> 4, q4 = (l & 15) >> 2, p4 = l & 3, blk = (l >> 4) & 1;
  const u32 xso = (128 * m + 32 * r + 16 * c) * 1152;
  const u32x4 sWS = make_srd(ws, WS_END);
  u32x4 xr[18];
  u32x2 ep; u32 braw;
  {
    const u32x4 sX = make_srd(x, 2048 * 1152);
    const u32 vo = (l >> 2) * 1152 + p4 * 16;
#define XL(i) xr[i] = ld16<64 * (i)>(sX, vo, xso);
    XL(0) XL(1) XL(2) XL(3) XL(4) XL(5) XL(6) XL(7) XL(8) XL(9) XL(10) XL(11) XL(12) XL(13) XL(14) XL(15) XL(16) XL(17)
#undef XL
    asm volatile("buffer_load_dwordx2 %0, %1, %2, 0 offen sc1" : "=v"(ep) : "v"(WS_EPOCH), "s"(sWS) : "memory");
    asm volatile("buffer_load_dword %0, %1, %2, 0 offen" : "=v"(braw) : "v"(tid < 192 ? 2304 * kq + 512 * s + 4 * tid : OOB), "s"(make_srd(b1, 9216)) : "memory");
  }
  {
    u64 pc; asm volatile("s_getpc_b64 %0" : "=s"(pc));
    touch(make_srd((const void*)(pc & ~127ull), 26624), l < 1 ? ((bid >> 3) * 8 + w) * 128 : OOB, LB + L_DUMP);
  }
  if (w == 7) {
    touch(sWS, l < PF_TLB_N ? 65536 + ((bid >> 3) * PF_TLB_N + l) * PF_TLB_STRIDE : OOB, LB + L_DUMP);
    touch(make_srd(out, 2048 * 1152), l < 2 ? ((bid >> 3) * 2 + l) * 36864 : OOB, LB + L_DUMP);
  }
  STAMP(21);
  const bool isq = s < 3;
  const int NC = isq ? 7 : 6, NQ = isq ? 3 : 0;
  const u32x4 sW1 = make_srd(w1, 576 * 9216);
  const u32 pcs = 16 * ((l & 7) ^ (4 * blk));
  const u32 vo_f = (l >> 3) * 9216 + pcs;
  u32 dl = LB + w * 3072;
  const u32 fcol = 2304 * kq + 512 * s;
  u32x4 sA = isq ? make_srd(wqkv, 576 * 3456) : sW1;
  u32 voA = isq ? (l >> 3) * 3456 + pcs : vo_f, i8 = isq ? 8 * 3456 : 8 * 9216, i176 = 22 * i8;
  u32 so = 3 * w * i8 + (isq ? 1152 * s + 256 * kq : fcol) - i176;
#define DMA_A(i, lds) dma16s<(i) * 1024>(sA, voA, so, (i) == 0 ? i176 : i8, lds)

  if (w == 0) {
    u32x4 mk[2];
#pragma unroll
    for (int e = 0; e < 8; ++e) { const int d = (l & 31) - 4 * h - ((2 * e & 3) + 8 * (e >> 1)); mk[e >> 2][e & 3] = d < 0 ? 0u : (d == 0 ? 0xffffu : ~0u); }
    lds_w16(LB + L_MSK + l * 16, mk[0]); lds_w16(LB + L_MSK + 1024 + l * 16, mk[1]);
  }
  u32 tv0, tv1;
  {
    const int trow = ((m & 7) * 8 + w) * 9 + l % 9, ln = l / 9;
    tv0 = ln < 2 ? trow * (i8 >> 3) + (isq ? 1152 * s + 256 * kq : fcol) + 128 * (1 + ln) : OOB;
    tv1 = ln < (isq ? 4 : 3) ? trow * 9216 + fcol + 128 * (ln + (isq ? 0 : 3)) : OOB;
    pin(tv0); pin(tv1);
  }
  u64 tag;
  u32x4 xf[36];
  float rs;
  {
    u32 xwa = LB + 3 * SLOT_A + r * 18432 + p4 * 512 + (((16 * c + (l >> 2)) ^ (p4 << 1)) << 4), xra = LB + 3 * SLOT_A + r * 18432 + h * 512 + (((l & 31) ^ (h << 1)) << 4), xrb = xra ^ 64;
    pin(xwa); pin(xra); pin(xrb);
    float s0 = 0.f, s1 = 0.f, q0 = 0.f, q1 = 0.f;
    auto sq = [&](int i) { q0 = dot2(xr[i][0], xr[i][0], q0); q1 = dot2(xr[i][1], xr[i][1], q1); q0 = dot2(xr[i][2], xr[i][2], q0); q1 = dot2(xr[i][3], xr[i][3], q1); };
#pragma unroll
    for (int T = 0; T < 6; ++T) {
      if (T == 0) wait_vm<17 + NPF>(); else if (T == 1) wait_vm<14 + NPF>(); else if (T == 2) wait_vm<11 + NPF>(); else if (T == 3) wait_vm<8 + NPF>(); else if (T == 4) wait_vm<5 + NPF>(); else wait_vm<2 + NPF>();
      if (T == 0) STAMP(22);
#pragma unroll
      for (int i = 3 * T; i < 3 * T + 3; ++i) {
        pin(xr[i]);
        s0 = dot2(xr[i][0], 0x3f803f80u, s0); s1 = dot2(xr[i][1], 0x3f803f80u, s1); s0 = dot2(xr[i][2], 0x3f803f80u, s0); s1 = dot2(xr[i][3], 0x3f803f80u, s1);
      }
      fence_sched();
    }
    STAMP(1);
    const float mean = quad_sum(s0 + s1) * (1.f / 576.f), nmean = -rbf(mean);
    auto centre = [&](int i) {
      float d0, d1, d2, d3, d4, d5, d6, d7; u32 o0, o1, o2, o3;
      asm volatile("s_nop 1\n\tv_dot2_f32_bf16 %4, %12, %17, %16\n\tv_dot2_f32_bf16 %5, %12, %18, %16\n\tv_dot2_f32_bf16 %6, %13, %17, %16\n\tv_dot2_f32_bf16 %7, %13, %18, %16\n\t"
                   "v_dot2_f32_bf16 %8, %14, %17, %16\n\tv_dot2_f32_bf16 %9, %14, %18, %16\n\tv_dot2_f32_bf16 %10, %15, %17, %16\n\tv_dot2_f32_bf16 %11, %15, %18, %16\n\t"
                   "v_cvt_pk_bf16_f32 %0, %4, %5\n\tv_cvt_pk_bf16_f32 %1, %6, %7\n\tv_cvt_pk_bf16_f32 %2, %8, %9\n\tv_cvt_pk_bf16_f32 %3, %10, %11"
                   : "=v"(o0), "=v"(o1), "=v"(o2), "=v"(o3), "=&v"(d0), "=&v"(d1), "=&v"(d2), "=&v"(d3), "=&v"(d4), "=&v"(d5), "=&v"(d6), "=&v"(d7)
                   : "v"(xr[i][0]), "v"(xr[i][1]), "v"(xr[i][2]), "v"(xr[i][3]), "v"(nmean), "s"(0x00003f80u), "s"(0x3f800000u));
      xr[i] = u32x4{o0, o1, o2, o3};
    };
#pragma unroll
    for (int i = 0; i < 9; ++i) {
      centre(i); lds_w16(xwa + i * 2048, xr[i]);
      if (i % 3 == 0) DMA_A(0, dl + (i / 3) * SLOT_A); else if (i % 3 == 1) DMA_A(1, dl + (i / 3) * SLOT_A); else DMA_A(2, dl + (i / 3) * SLOT_A);
    }
    so += 128 - 72 * i8;
    wait_lgkm0(); bar();
    STAMP(23);
#pragma unroll
    for (int i = 0; i < 9; ++i) {
      xf[2 * i] = lds_r16(xra + 2 * i * 1024); xf[2 * i + 1] = lds_r16(xrb + (2 * i + 1) * 1024);
      centre(9 + i);
      fence_sched();
    }
    touch(sA, tv0, LB + L_DUMP); touch(sW1, tv1, LB + L_DUMP);
    wait_lgkm0(); bar();
    STAMP(24);
#pragma unroll
    for (int i = 0; i < 9; ++i) { lds_w16(xwa + i * 2048, xr[9 + i]); sq(i); sq(9 + i); fence_sched(); }
    {
      const float dm = mean + nmean;
      const float var = __builtin_fmaxf(quad_sum(q0 + q1) * (1.f / 576.f) - dm * dm, 0.f);
      const float rsd = __builtin_amdgcn_rcpf(rbf(__builtin_amdgcn_sqrtf(rbf(rbf(var) + 1e-5f))));
      if (p4 == 0) *(LAS float*)(LB + L_RS + (32 * r + 16 * c + (l >> 2)) * 4) = rsd;
      wait_vm<5>();
      pin(ep); pin(braw);
      if (tid < 256) *(LAS f32x2*)(LB + L_BIAS + tid * 8) = f32x2{blo(braw), bhi(braw)};
      tag = (((u64)(u32)__builtin_amdgcn_readfirstlane(ep[1]) << 32) | (u32)__builtin_amdgcn_readfirstlane(ep[0])) + 1;
    }
    wait_lgkm0(); bar();
#pragma unroll
    for (int i = 0; i < 9; ++i) { xf[18 + 2 * i] = lds_r16(xra + 2 * i * 1024); xf[19 + 2 * i] = lds_r16(xrb + (2 * i + 1) * 1024); }
    rs = *(LAS float*)(LB + L_RS + (32 * r + (l & 31)) * 4);
  }
  STAMP(2);

  const u32x4 sG = make_srd(ws + WS_G + (kq * 16 + m) * G_SZ, G_SZ);
  f32x16 prev = {};
  u32 ba = LB + L_BIAS + c * 128 + h * 16;
  u32 hso = (8 * s + c) * 8192, gvo;
  {
    const u32 wl = LB + h * 1024 + q4 * 128 + 64 * (c ^ ((q4 >> 1) & 1)) + 32 * blk + 8 * p4;
    constexpr int PF = 4;
    bf16x8 wf[PF];
#pragma unroll
    for (int j = 0; j < PF; ++j) wf[j] = tr_frag(wl + j * 2048, wl + j * 2048 + 512);
    u32 R0 = 0, R1 = SLOT_A, R2 = 2 * SLOT_A, R3 = 3 * SLOT_A;
    f32x16 acc;
    auto kstep = [&](auto KK) __attribute__((always_inline)) {
      constexpr int kk = decltype(KK)::v, sl = kk / 12, ks = kk % 12;
      if constexpr (kk == 0) acc = MFMA32(wf[0], as_bf(xf[0]), f32x16{});
      else if constexpr (kk < 35) acc = MFMA32(wf[ks % PF], as_bf(xf[kk]), acc);
      else asm volatile("v_mfma_f32_32x32x16_bf16 %0, %1, %2, %3" : "=&v"(prev) : "v"(wf[ks % PF]), "v"(xf[kk]), "v"(acc));
      if constexpr (kk < 35) pin(acc);
      fence_sched();
      constexpr int sn = (kk + PF) / 12;
      const u32 pa = wl + (sn == 0 ? R0 : sn == 1 ? R1 : sn == 2 ? R2 : R3) + ((ks + PF) % 12) * 2048;
      wf[ks % PF] = tr_frag(pa, pa + 512);
      fence_sched();
      const u32 dst = dl + (sl == 0 ? R3 : sl == 1 ? R0 : R1);
      if constexpr (ks == 1) DMA_A(0, dst);
      if constexpr (ks == 5) DMA_A(1, dst);
      if constexpr (ks == 9) DMA_A(2, dst);
    };
    wait_lgkm0(); bar();
    STAMP(3);
    {
      const u32 qvo = h * 1024 + (32 * r + (l & 31)) * 8;
      const u32x4 sQ = make_srd(ws + WS_QKV + (kq * 16 + m) * QKV_SZ + s * 36864 - 32768, isq ? 36864 + 32768 : 0);
      int oq = (32 * c - 16 * kq - 64) * 256;
      const u32x4 sW2 = make_srd(w2 + 1152 * kq * 288, 1152 * 1152);
      u32 tv2 = (isq && l < 14) ? ((((m & 7) * 3 + s) * 8 + w) * 56 + l) * 128 : OOB;
      const float re = s == 0 ? rs * 0.34004557f : rs;
#pragma unroll 1
      for (int ci = 0; ci <= NQ; ++ci) {
        u32x2 un;
        sfor<0, 36>([&](auto KK) __attribute__((always_inline)) {
          constexpr int kk = decltype(KK)::v;
          kstep(KK);
          if constexpr (kk == 2 || kk == 3 || kk == 6 || kk == 7 || kk == 14 || kk == 15 || kk == 18 || kk == 19) {
            constexpr int e = (kk & 1) + ((kk >> 2) & 1) * 2 + (kk >= 14 ? 4 : 0) - (kk >= 14 ? (((kk >> 2) & 1) * 2 - (kk >= 18 ? 2 : 0)) : 0);
            u32 v = pk2(prev[2 * e] * re, prev[2 * e + 1] * re); pin(v); un[e & 1] = v;
          }
          if constexpr (kk == 4 || kk == 10 || kk == 16 || kk == 22) {
            const int o = kk < 12 ? oq : oq + 4096;
            if constexpr (kk == 4 || kk == 16) st8c<0>(un, sQ, qvo, o < 0 ? OOB : (u32)o + 32768); else st8c<2048>(un, sQ, qvo, o < 0 ? OOB : (u32)o + 32768);
          }
          if constexpr (kk == 34) {
            if (RARE(ci == 0)) {
              int b_ = bid, t_ = tid; pins(b_); pin(t_);
              u32x4 sv = {SENT, SENT, SENT, SENT}; pin(sv);
#pragma unroll
              for (int i = 0; i < 4; ++i) st16c<0>(sv, sWS, (i < 3 || t_ < 192) ? t_ * 16 + i * 8192 : OOB, WS_P + b_ * 27648);
            }
          }
          if constexpr (kk == 34) {
            touch(sW2, tv2, LB + L_DUMP); tv2 += 14 * 128;
            oq += 16384;
            so += 128 - 72 * i8;
            if (RARE(ci + 2 == NQ)) { sA = sW1; voA = vo_f; i8 = 8 * 9216; i176 = 176 * 9216; so = 24 * w * 9216 + fcol - 176 * 9216; }
          }
          fence_sched();
          if constexpr (kk == 35) { const u32 t = R3; R3 = R2; R2 = R1; R1 = R0; R0 = t; }
          if constexpr (kk % 12 == 11) { if constexpr (kk == 11) wait_vm<6>(); else if constexpr (kk == 23) wait_vm<6>(); else wait_vm<5>(); bar(); }
        });
      }
    }
    STAMP(4);
    gvo = 2 * r * 1024 + blk * 1024 + h * 256 + t16 * 16;
    f32x4 bq = *(LAS f32x4*)ba;
#pragma unroll 1
    for (int ci = NQ + 1;; ++ci) {
      u32x4 un[2];
      float xa, xb, ea, eb;
      const bool ev = ci == 4 || ci == NC - 1;
      sfor<0, 36>([&](auto KK) __attribute__((always_inline)) {
        constexpr int kk = decltype(KK)::v;
        kstep(KK);
        if constexpr (kk >= 2 && kk < 34) {
          constexpr int t = kk - 2, e = 2 * (t >> 2);
          if constexpr ((t & 3) == 0) { xa = __builtin_fmaf(prev[e], rs, bq[e & 3]); ea = gelu_a(xa); pin(ea); }
          if constexpr ((t & 3) == 1) { xb = __builtin_fmaf(prev[e + 1], rs, bq[(e & 3) + 1]); eb = gelu_a(xb); pin(eb); }
          if constexpr ((t & 7) == 5) { if constexpr (t == 29) ba += 256; bq = *(LAS f32x4*)(ba + (((t >> 3) + 1) & 3) * 32); }
          if constexpr ((t & 3) == 2) { ea = gelu_b(xa, ea); pin(ea); }
          if constexpr ((t & 3) == 3) { u32 v = pk2(ea, gelu_b(xb, eb)); pin(v); un[t >> 4][(t >> 2) & 3] = v; }
        }
        if constexpr (kk == 0) {
          if (RARE(ev)) {
            if (ci == 4) wait_vm<0>();
            else {
              int m_ = m, w_ = w, t_ = tid; pins(m_); pins(w_); pin(t_);
              const int l_ = (t_ >> 1) & 7, pm = l_ < 3 ? m_ : (l_ < 5 ? m_ - 1 : m_ - 2);
              asm volatile("s_mov_b32 m0, %0\n\ts_mov_b64 exec, 0xffff\n\tbuffer_load_dword %1, %2, 0 offen sc1 lds\n\ts_mov_b64 exec, -1"
                           :: "s"(LB + L_FLG + w_ * 64), "v"(WS_FLAG + bid_of(pm < 0 ? m_ : pm, kq, l_ < 3 ? l_ : 1 + (l_ & 1)) * 128 + (t_ & 1) * 4), "s"(sWS) : "memory");
            }
          }
        }
        if constexpr (kk == 12) {
          if (RARE(ev)) {
            int m_ = m, w_ = w, b_ = bid, t_ = tid; pins(m_); pins(w_); pins(b_); pin(t_);
            if (ci == 4) { if (t_ == 0) __hip_atomic_store((GAS u64*)(ws + WS_FLAG + b_ * 128), tag, RLX_AGENT); }
            else {
            wait_vm<3>();
            const u32x2 fl = lds_r8(LB + L_FLG + w_ * 64 + (t_ & 7) * 8);
            const int l_ = t_ & 7, pm = l_ < 3 ? m_ : (l_ < 5 ? m_ - 1 : m_ - 2);
            if (!all_lanes(fl[0] == (u32)tag && fl[1] == (u32)(tag >> 32))) poll_words(ws, WS_FLAG + bid_of(pm < 0 ? m_ : pm, kq, l_ < 3 ? l_ : 1 + (l_ & 1)) * 128, true, tag);
            const u32 src = WS_QKV + (kq * 16 + m_ - 2) * QKV_SZ + (36 + w_) * 1024 + 9216 * s, dd = LB + L_KV + w_ * PP, lv = (t_ & 63) * 16;
            const u32 s0 = m_ >= 2 ? src : OOB, s1 = m_ >= 1 ? src + QKV_SZ : OOB, s2 = src + 2 * QKV_SZ;
            dma16c<0, 0>(sWS, lv, s0, dd); dma16c<9 * PP, 36864>(sWS, lv, s0, dd);
            dma16c<1024, 0>(sWS, lv, s1, dd); dma16c<9 * PP + 1024, 36864>(sWS, lv, s1, dd);
            dma16c<2048, 0>(sWS, lv, s2, dd); dma16c<9 * PP + 2048, 36864>(sWS, lv, s2, dd);
            if (w_ < 6) {
              const int mt = w_ >> 1, kv = w_ & 1;
              dma16c<0, 0>(sWS, lv, (mt == 0 ? s0 : mt == 1 ? s1 : s2) + (8 - w_) * 1024 + kv * 36864, LB + L_KV + (8 + 9 * kv) * PP + mt * 1024);
              *(LAS u32x2*)(LB + L_KV + 18 * PP + t_ * 8) = u32x2{t_ < 256 - 128 * m_ ? 0xff7f3f80u : 0x00003f80u, 0x3f803f80u};
            }
            }
          }
        }
        if constexpr (kk == 22) st16c<0>(un[0], sG, gvo, hso);
        if constexpr (kk == 34) {
          st16c<512>(un[1], sG, gvo, hso);
          hso += 16384;
          so += 128 - 576 * 9216;
          if (RARE(ci + 2 == NC)) { sA[2] = 0; dl = L_TOTAL; }
        }
        fence_sched();
        if constexpr (kk == 35) { const u32 t = R3; R3 = R2; R2 = R1; R1 = R0; R0 = t; }
        if constexpr (kk == 11) { wait_vm<4>(); bar(); }
        if constexpr (kk == 23) { if (ci != NC - 1) { wait_vm<4>(); bar(); } }
      });
      if (ci == NC - 1) break;
      wait_vm<5>(); bar();
    }
  }
  STAMP(10);
  const auto rWS = __builtin_amdgcn_make_buffer_rsrc(ws, 0, WS_END, 0x00020000);
  const auto rG = __builtin_amdgcn_make_buffer_rsrc(ws + WS_G + (kq * 16 + m) * G_SZ, 0, G_SZ, 0x00020000);
  const u32x4 sB = make_srd(w2 + 1152 * kq * 288, 1152 * 1152);
  u32x4 qf, qt = {};
  {
    const u32 qo = WS_QKV + (kq * 16 + m) * QKV_SZ + 9216 * s, qv = (32 * r + (l & 31)) * 8;
    qf = cat(__builtin_amdgcn_raw_buffer_load_b64(rWS, qv + (5 * c + 2 * h) * 1024, qo, 16), __builtin_amdgcn_raw_buffer_load_b64(rWS, qv + (5 * c + 2 * h + 1) * 1024, qo, 16));
    const u32 qq = __builtin_amdgcn_raw_buffer_load_b32(rWS, h ? OOB : qv + 4096 + 4 * c, qo, 16);
    qt[0] = c ? 0u : qq; qt[1] = c ? qq : 0u; qt[2] = h ? 0u : 0x3f800000u;
  }
  u32 LG[4];
#pragma unroll
  for (int i = 0; i < 4; ++i) { const int g = (i + s) & 3; LG[i] = g == 3 ? 14 : 4 * g; }
  const u32 gl0 = c * 8192 + (2 * r + c) * 1024 + l * 16, gl1 = gl0 ^ 1024;
  u32x4 gf[6][2];
#define G_OFF(n) ((n) < 16 ? (LG[((n) >> 2) & 3] + ((n) & 3)) * 16384 : (u32)((n) < 18 ? (n) - 4 : (n)) * 16384)
#define ISSUE_G(n, e) gf[(n) % 6][e] = __builtin_amdgcn_raw_buffer_load_b128(rG, (e) ? gl1 : gl0, G_OFF(n), 16)
  ISSUE_G(0, 0); ISSUE_G(0, 1); ISSUE_G(1, 0); ISSUE_G(1, 1);
  u32 vb[3];
#pragma unroll
  for (int i = 0; i < 3; ++i) { const int pc = 64 * (w + 8 * i) + l, pp = pc % 18; vb[i] = (pc / 18) * 1152 + (pp < 16 ? pp ^ (4 * kq) : pp) * 16 + 288 * s; }
  const u64 m3 = __builtin_amdgcn_ballot_w64(tid < 128);
  const u32 db = LB + w * 1024;
  u32 WG[4];
#pragma unroll
  for (int i = 0; i < 4; ++i) WG[i] = LG[i] * 73728;
  auto issue_w = [&](auto N_, auto I_) __attribute__((always_inline)) {
    constexpr int n = decltype(N_)::v, i = decltype(I_)::v, LI = (n % 5) * SLOT_B + i * 8192, SI = (n < 16 ? (n & 3) : n - 4) * 73728;
    const u32 base = n < 16 ? WG[(n >> 2) & 3] : 0u;
    if constexpr (i < 2) dma16<LI, SI>(sB, vb[i], base, db); else dma16m<LI, SI>(sB, vb[2], base, db, m3);
  };
#define ISSUE_W(n, i) issue_w(IC<(n)>{}, IC<(i)>{})
  {
    asm volatile("s_nop 7\n\ts_nop 3" ::: "memory");
    u32x4 un[2];
    sfor<0, 4>([&](auto G_) __attribute__((always_inline)) {
      constexpr int gq = decltype(G_)::v;
      const f32x4 b = *(LAS f32x4*)(ba + gq * 32);
      float y[4];
#pragma unroll
      for (int e = 0; e < 4; ++e) { const float xx = __builtin_fmaf(prev[4 * gq + e], rs, b[e]); y[e] = gelu_b(xx, gelu_a(xx)); }
      un[gq >> 1][2 * (gq & 1)] = pk2(y[0], y[1]); un[gq >> 1][2 * (gq & 1) + 1] = pk2(y[2], y[3]);
      if constexpr (gq & 1) pin(un[gq >> 1]);
    });
    pin(qf); pin(qt);
    wait_lgkm0(); bar();
    st16c<0>(un[0], sG, gvo, hso); st16c<512>(un[1], sG, gvo, hso);
    ISSUE_G(2, 0); ISSUE_G(2, 1);
#pragma unroll
    for (int e = 0; e < 4; ++e) {
      auto a = __builtin_amdgcn_permlane32_swap(un[0][e], un[1][e], false, false);
      auto b = __builtin_amdgcn_permlane16_swap(a[0], a[1], false, false);
      gf[3][0][e] = c ? b[1] : b[0]; gf[3][1][e] = c ? b[0] : b[1];
    }
  }
  STAMP(11);
  u64 hfl;
  {
    u32 ka = LB + L_KV + (32 * r + (l & 31)) * 8, kb = ka + 4 * PP;
    ka += (5 * c + 2 * h) * PP;
    u32 va = LB + L_KV + (32 * r + 4 * h + q4) * 8 + (blk ? (p4 ? 18 : 13) : 9 + 5 * c + p4) * PP;
    pin(ka); pin(kb); pin(va);
#define K_MAIN(i) as_bf(cat(lds_r8(ka + (i) * 256), lds_r8(ka + PP + (i) * 256)))
#define K_TAIL(i) as_bf(cat(lds_r8(kb + (i) * 256), lds_r8(kb + 14 * PP + (i) * 256)))
    bf16x8 km[3], kt[3], k8;
    f32x16 st[9];
    km[0] = K_MAIN(0); kt[0] = K_TAIL(0); km[1] = K_MAIN(8); kt[1] = K_TAIL(8); k8 = kt[1];
    float mx0, mx1;
    sfor<0, 11>([&](auto I_) __attribute__((always_inline)) {
      constexpr int ii = decltype(I_)::v;
      if constexpr (ii + 2 < 9) { km[(ii + 2) % 3] = K_MAIN(ii + 1); kt[(ii + 2) % 3] = K_TAIL(ii + 1); }
      if constexpr (ii < 9) {
        st[ii] = MFMA32(km[ii % 3], as_bf(qf), f32x16{});
        st[ii] = MFMA32(kt[ii % 3], as_bf(qt), st[ii]);
        pin(st[ii]);
      }
      if constexpr (ii < 6) ISSUE_W(ii / 3, ii % 3);
      if constexpr (ii == 3) {
#pragma unroll
        for (int e = 0; e < 16; ++e) st[1][e] = __builtin_amdgcn_inverse_ballot_w64(diag_mask(e)) ? st[1][e] : st[0][e];
        mx0 = st[1][0]; mx1 = st[1][1];
      }
      if constexpr (ii >= 3) {
        constexpr int i = ii - 2;
#pragma unroll
        for (int e = (ii == 3 ? 2 : 0); e < 16; e += 2) { if (e & 2) mx0 = max3(mx0, st[i][e], st[i][e + 1]); else mx1 = max3(mx1, st[i][e], st[i][e + 1]); }
        pin(mx0); pin(mx1);
      }
      fence_sched();
    });
    float mx;
    { auto sw = __builtin_amdgcn_permlane32_swap(f2u(mx0), f2u(mx1), false, false); mx = max3(u2f(sw[0]), u2f(sw[1]), u2f(sw[0]));
      auto s2 = __builtin_amdgcn_permlane32_swap(f2u(mx), f2u(mx), false, false); mx = max3(u2f(s2[0]), u2f(s2[1]), u2f(s2[0])); }
    u32x4 qm = {};
    { const float n0 = -mx, a0 = bhi(f2u(n0)), n1 = n0 - a0, a1 = bhi(f2u(n1)), n2 = n1 - a1;
      qm[2] = h ? 0u : f2u(a0) >> 16; qm[3] = h ? 0u : ((f2u(a1) >> 16) | (f2u(n2) & 0xffff0000u)); }
    st[1] = MFMA32(k8, as_bf(qm), st[1]); st[2] = MFMA32(k8, as_bf(qm), st[2]); pin(st[1]); pin(st[2]);
    bf16x8 vf[2], v0[2];
    vf[0] = tr_frag(va + 8 * 256, va + 8 * 256 + 64);
    v0[0] = tr_frag(va, va + 64); v0[1] = tr_frag(va + 128, va + 192);
    const u32x4 mk0 = lds_r16(LB + L_MSK + l * 16), mk1 = lds_r16(LB + L_MSK + 1024 + l * 16);
    STAMP(12);
    fence_sched(); wait_vm<0>(); bar();
    STAMP(13);
    if (tid == 0) __hip_atomic_store((GAS u64*)(ws + WS_FLAG + (256 + bid) * 128), tag, RLX_AGENT);
    f32x16 o;
    sfor<0, 16>([&](auto J_) __attribute__((always_inline)) {
      constexpr int j = decltype(J_)::v, i = 1 + (j >> 1), u = j & 1, jn = j + 1, tn = (jn >> 1) == 0 ? 8 : (jn >> 1);
      if constexpr (jn < 16) vf[jn & 1] = tr_frag(va + tn * 256 + (jn & 1) * 128, va + tn * 256 + (jn & 1) * 128 + 64);
      if constexpr (u == 0 && i + 2 < 9) { st[i + 2] = MFMA32(k8, as_bf(qm), st[i + 2]); pin(st[i + 2]); }
      u32x4 pf;
#pragma unroll
      for (int e = 0; e < 4; ++e) pf[e] = pk2(__builtin_amdgcn_exp2f(st[i][8 * u + 2 * e]), __builtin_amdgcn_exp2f(st[i][8 * u + 2 * e + 1]));
      if constexpr (i == 1) {
        const u32x4 mk = u ? mk1 : mk0;
        if constexpr (j == 0) o = MFMA32(v0[u], as_bf(pf & ~mk), f32x16{}); else o = MFMA32(v0[u], as_bf(pf & ~mk), o);
        pf &= mk;
      }
      o = MFMA32(vf[u], as_bf(pf), o); pin(o);
      if constexpr (j >= 1 && j < 10) ISSUE_W(2 + (j - 1) / 3, (j - 1) % 3);
      if constexpr (j == 12) hfl = __builtin_bit_cast(u64, __builtin_amdgcn_raw_buffer_load_b64(rWS, (l & 3) * 1024, WS_FLAG + (256 + (bid & ~24)) * 128, 16));
      fence_sched();
    });
    const float inv = __builtin_amdgcn_rcpf(h ? o[8] : o[12]);
    if (RARE(!all_lanes(hfl == tag))) poll_words(ws, WS_FLAG + (256 + (bid & ~24)) * 128 + (l & 3) * 1024, true, tag);
    ISSUE_G(4, 0); ISSUE_G(4, 1);
    st16c<0>(u32x4{pk2(o[0] * inv, o[1] * inv), pk2(o[2] * inv, o[3] * inv), pk2(o[4] * inv, o[5] * inv), pk2(o[6] * inv, o[7] * inv)}, sG, gvo, (36 + s) * 8192 + c * 512);
    st16c<0>(u32x4{c ? pk2(o[10] * inv, o[11] * inv) : pk2(o[8] * inv, o[9] * inv), 0u, 0u, 0u}, sG, h ? OOB : gvo, (40 + (s >> 1)) * 8192 + (2 * (s & 1) + c) * 256);
  }
  STAMP(14);
  f32x4 ac[2][9];
  {
    u32 wb = LB + c * 9216 + (16 * (g4 >> 1) + 4 * (g4 & 1) + q4) * 288 + 8 * p4, wb2 = wb + 3 * SLOT_B, wo1, wo2;
    pin(wb); pin(wb2);
    bf16x8 af[9];
#pragma unroll
    for (int d = 0; d < 8; ++d) af[d] = tr_frag(wb + d * 32, wb + d * 32 + 2304);
    STAMP(15);
    sfor<0, 189>([&](auto Q_) __attribute__((always_inline)) {
      constexpr int q = decltype(Q_)::v, b = q / 9, d = q % 9;
      constexpr int qn = q + 8, bn = qn / 9, dn = qn % 9, rb = bn % 5;
      const u32 pa = (bn < 18 ? (rb < 3 ? wb + rb * SLOT_B : wb2 + (rb - 3) * SLOT_B) : bn < 20 ? wo1 + (bn - 18) * 72 * 288 : wo2) + dn * 32;
      s16x4 lo, hi;
      if constexpr (b == 0) ac[0][d] = MFMA16(af[d], as_bf(gf[b % 6][0]), f32x4{}); else ac[0][d] = MFMA16(af[d], as_bf(gf[b % 6][0]), ac[0][d]);
      pin(ac[0][d]); fence_sched();
      if constexpr (bn < 21) lo = __builtin_amdgcn_ds_read_tr16_b64_v4i16((LAS s16x4*)pa);
      fence_sched();
      if constexpr (b == 0) ac[1][d] = MFMA16(af[d], as_bf(gf[b % 6][1]), f32x4{}); else ac[1][d] = MFMA16(af[d], as_bf(gf[b % 6][1]), ac[1][d]);
      pin(ac[1][d]); fence_sched();
      if constexpr (bn < 21) { hi = __builtin_amdgcn_ds_read_tr16_b64_v4i16((LAS s16x4*)(pa + 2304)); af[dn] = __builtin_bit_cast(bf16x8, __builtin_shufflevector(lo, hi, 0, 1, 2, 3, 4, 5, 6, 7)); }
      fence_sched();
      if constexpr (b >= 1 && b + 4 < 18) { if constexpr (d == 1) ISSUE_W(b + 4, 0); if constexpr (d == 4) ISSUE_W(b + 4, 1); if constexpr (d == 6) ISSUE_W(b + 4, 2); }
      if constexpr (b + 5 < 21) { if constexpr (d == 2) ISSUE_G(b + 5, 0); if constexpr (d == 7) ISSUE_G(b + 5, 1); }
      if constexpr (q == 27) wait_vm<0>();
      if constexpr (q == 36) { if (tid == 0) __hip_atomic_store((GAS u64*)(ws + WS_FLAG + (512 + bid) * 128), tag, RLX_AGENT); }
      if constexpr (q == 90) hfl = __builtin_bit_cast(u64, __builtin_amdgcn_raw_buffer_load_b64(rWS, (l & 4) ? ((bid & ~3) + (l & 3)) * 128 : (512 + (bid & ~24)) * 128 + (l & 3) * 1024, WS_FLAG, 16));
      if constexpr (q == 117) {
        if (RARE(!all_lanes(hfl == tag))) poll_words(ws, WS_FLAG + ((l & 4) ? ((bid & ~3) + (l & 3)) * 128 : (512 + (bid & ~24)) * 128 + (l & 3) * 1024), true, tag);
      }
      if constexpr (b == 14 || b == 15) {
        const u32x4 sWO = make_srd(wo + 144 * kq * 288, 144 * 1152);
        if constexpr (d == 0) dma16<L_KV + (b - 14) * SLOT_B, (b - 14) * 73728>(sWO, vb[0], 0u, db);
        if constexpr (d == 3) dma16<L_KV + (b - 14) * SLOT_B + 8192, (b - 14) * 73728>(sWO, vb[1], 0u, db);
        if constexpr (d == 5) dma16m<L_KV + (b - 14) * SLOT_B + 16384, (b - 14) * 73728>(sWO, vb[2], 0u, db, m3);
        if constexpr (b == 15 && d == 8) { if (w < 6) dma16<L_KV + 2 * SLOT_B, 2 * 73728>(sWO, vb[0], 0u, db); }
      }
      if constexpr (q == 145) {
        wo1 = LB + L_KV + (36 * c + 20 * (g4 >> 1) + 4 * (g4 & 1) + q4) * 288 + 8 * p4;
        wo2 = LB + L_KV + (72 * c + 36 * (g4 >> 1) + 2 * (g4 & 1) + 16 + q4) * 288 + 8 * p4;
        pin(wo1); pin(wo2);
      }
      if constexpr (q == 155) {
        const u32x4 sX = make_srd(x, 2048 * 1152);
        dma16<L_RES, 0>(sX, (l >> 2) * 1152 + p4 * 16 + 288 * s + 64 * kq, xso, LB + (2 * r + c) * 1024);
        if (w == 0) dma16<L_RES + 8192, 0>(sX, (l >> 1) * 1152 + (l & 1) * 16 + 288 * s + 256, (128 * m + 32 * kq) * 1152, LB);
        if (w == 1) dma16<L_RES + 9216, 0>(make_srd(b2, 1152), l * 16 + 288 * s, 0, LB);
        if (bid == 24 && w == 4) {
#pragma unroll 1
          for (int i = 0; i < 4; ++i) poll_words(ws, WS_FLAG + (l + 64 * i) * 128, true, tag);
          if (l == 0) __hip_atomic_store((GAS u64*)(ws + WS_EPOCH), tag, RLX_AGENT);
        }
      }
      if constexpr (b == 20 && d >= 1) *(LAS f32x4*)(LB + w * 9216 + l * 16 + (d - 1) * 1024) = ac[1][d - 1];
      fence_sched();
      if constexpr (d == 8 && b < 19) {
        if constexpr (b == 0) wait_vm<6>(); else if constexpr (b >= 3 && b <= 15) wait_vm<8>(); else if constexpr (b == 16) wait_vm<0>();
        if constexpr (b != 17) bar();
        STAMP(41 + b);
      }
    });
  }
  STAMP(16);
  {
    *(LAS f32x4*)(LB + w * 9216 + l * 16 + 8 * 1024) = ac[1][8];
    wait_vm<0>(); wait_lgkm0(); bar();
    f32x4 v[9];
#pragma unroll
    for (int d = 8; d >= 0; --d) v[d] = ac[0][d] + *(LAS f32x4*)(LB + (w ^ 4) * 9216 + l * 16 + d * 1024);
#pragma unroll
    for (int k = 3; k >= 1; --k)
      st16c<0>(u32x4{umin(pk2(v[2 * k][0], v[2 * k][1]), SENT - 1), umin(pk2(v[2 * k][2], v[2 * k][3]), SENT - 1), umin(pk2(v[2 * k + 1][0], v[2 * k + 1][1]), SENT - 1), umin(pk2(v[2 * k + 1][2], v[2 * k + 1][3]), SENT - 1)},
               sWS, l * 16, WS_P + (bid ^ k) * 27648 + (k - 1) * 9216 + w * 1024);
    const bool own8 = r == kq;
    st8c<0>(u32x2{umin(pk2(v[8][0], v[8][1]), SENT - 1), umin(pk2(v[8][2], v[8][3]), SENT - 1)}, sWS, l * 8, own8 ? OOB : WS_P + (bid ^ kq ^ r) * 27648 + ((kq ^ r) - 1) * 9216 + 8192 + c * 512);
    STAMP(19);
    const u32 ra = LB + L_RES + (2 * r + c) * 1024 + t16 * 64 + g4 * 8, r8 = LB + L_RES + 8192 + (16 * c + t16) * 32 + g4 * 8, rb = LB + L_RES + 9216 + g4 * 8;
    {
      const u32x2 x0 = lds_r8(ra), x1 = lds_r8(ra + 32), x8 = lds_r8(r8), b0 = lds_r8(rb + 64 * kq), b1_ = lds_r8(rb + 64 * kq + 32), b8 = lds_r8(rb + 256);
      v[0] += f32x4{blo(x0[0]), bhi(x0[0]), blo(x0[1]), bhi(x0[1])} + f32x4{blo(b0[0]), bhi(b0[0]), blo(b0[1]), bhi(b0[1])};
      v[1] += f32x4{blo(x1[0]), bhi(x1[0]), blo(x1[1]), bhi(x1[1])} + f32x4{blo(b1_[0]), bhi(b1_[0]), blo(b1_[1]), bhi(b1_[1])};
      v[8] += f32x4{blo(x8[0]), bhi(x8[0]), blo(x8[1]), bhi(x8[1])} + f32x4{blo(b8[0]), bhi(b8[0]), blo(b8[1]), bhi(b8[1])};
      pin(v[0]); pin(v[1]); pin(v[8]);
    }
    const u32x4 sOUT = make_srd(out, 2048 * 1152);
    u32 ov = (l >> 2) * 1152 + p4 * 16 + 288 * s + 64 * kq, ov8 = (own8 && l < 32) ? (l >> 1) * 1152 + (l & 1) * 16 + 288 * s + 256 : OOB;
    pin(ov); pin(ov8);
    const u32 po = WS_P + bid * 27648;
    u32x4 pa[3]; u32x2 pb[3];
#pragma unroll 1
    for (u32 spins = 0; spins < SPINS; ++spins) {
      asm volatile("" ::: "memory");
#pragma unroll
      for (int sg = 0; sg < 3; ++sg) {
        pa[sg] = __builtin_amdgcn_raw_buffer_load_b128(rWS, l * 16, po + sg * 9216 + w * 1024, 16);
        pb[sg] = __builtin_amdgcn_raw_buffer_load_b64(rWS, l * 8, own8 ? po + sg * 9216 + 8192 + c * 512 : OOB, 16);
      }
      u32 mxu = 0;
#pragma unroll
      for (int sg = 0; sg < 3; ++sg) mxu = umax(umax(umax(mxu, umax(pa[sg][0], pa[sg][1])), umax(pa[sg][2], pa[sg][3])), umax(pb[sg][0], pb[sg][1]));
      if (all_lanes(mxu != SENT)) break;
      __builtin_amdgcn_s_sleep(1);
    }
    STAMP(20);
#pragma unroll
    for (int sg = 0; sg < 3; ++sg) {
      v[0] += f32x4{blo(pa[sg][0]), bhi(pa[sg][0]), blo(pa[sg][1]), bhi(pa[sg][1])};
      v[1] += f32x4{blo(pa[sg][2]), bhi(pa[sg][2]), blo(pa[sg][3]), bhi(pa[sg][3])};
    }
    *(LAS u32x2*)ra = u32x2{pk2(v[0][0], v[0][1]), pk2(v[0][2], v[0][3])};
    *(LAS u32x2*)(ra + 32) = u32x2{pk2(v[1][0], v[1][1]), pk2(v[1][2], v[1][3])};
    const u32x4 t = lds_r16(LB + L_RES + (2 * r + c) * 1024 + l * 16);
    asm volatile("buffer_store_dwordx4 %0, %1, %2, %3 offen\n\ts_nop 1" :: "v"(t), "v"(ov), "s"(sOUT), "s"(xso) : "memory");
    if (own8) {
#pragma unroll
      for (int sg = 0; sg < 3; ++sg) v[8] += f32x4{blo(pb[sg][0]), bhi(pb[sg][0]), blo(pb[sg][1]), bhi(pb[sg][1])};
      *(LAS u32x2*)r8 = u32x2{pk2(v[8][0], v[8][1]), pk2(v[8][2], v[8][3])};
      const u32x4 t8 = lds_r16(LB + L_RES + 8192 + c * 512 + (l & 31) * 16);
      asm volatile("buffer_store_dwordx4 %0, %1, %2, %3 offen\n\ts_nop 1" :: "v"(t8), "v"(ov8), "s"(sOUT), "s"(xso) : "memory");
    }
  }
}
#if defined(__HIP_DEVICE_COMPILE__)
#pragma clang attribute pop
#endif

extern "C" void kernel_launch(void* const* d_in, const int* in_sizes, int n_in, void* d_out, int out_size, void* d_ws, size_t ws_size, hipStream_t stream) {
  fused_block<<<256, 512, 0, stream>>>((const u32*)d_in[0], (const u32*)d_in[1], (const u32*)d_in[2], (const u32*)d_in[4], (const u32*)d_in[5],
                                       (const u32*)d_in[6], (const u32*)d_in[7], (u32*)d_out, (char*)d_ws);
}
```

```cpp
#include <hip/hip_runtime.h>
#pragma clang diagnostic ignored "-Wint-to-pointer-cast"
#if defined(__HIP_DEVICE_COMPILE__)
#pragma clang attribute push (__attribute__((target("no-packed-fp32-ops"))), apply_to = function)
#endif
typedef unsigned u32;
typedef unsigned long long u64;
typedef short s16x4 __attribute__((ext_vector_type(4)));
typedef __bf16 bf16x8 __attribute__((ext_vector_type(8)));
typedef __bf16 bf16x2 __attribute__((ext_vector_type(2)));
typedef float f32x2 __attribute__((ext_vector_type(2)));
typedef float f32x4 __attribute__((ext_vector_type(4)));
typedef float f32x16 __attribute__((ext_vector_type(16)));
typedef u32 u32x2 __attribute__((ext_vector_type(2)));
typedef u32 u32x4 __attribute__((ext_vector_type(4)));
#define LAS __attribute__((address_space(3)))
#define GAS __attribute__((address_space(1)))
#define DEV __device__ __forceinline__
#define RLX_AGENT __ATOMIC_RELAXED, __HIP_MEMORY_SCOPE_AGENT

constexpr u32 WS_EPOCH = 0;
constexpr u32 WS_FLAG = 1024;
constexpr u32 QKV_SZ = 3 * 36 * 1024;
constexpr u32 WS_QKV = 262144;
constexpr u32 G_SZ = 42 * 8192;
constexpr u32 WS_G = WS_QKV + 64 * QKV_SZ;
constexpr u32 WS_P = WS_G + 64 * G_SZ;
constexpr u32 WS_END = WS_P + 256 * 27648;
constexpr u32 OOB = 0x7ffffff0u;
constexpr u32 PP = 3104;
constexpr u32 L_KV = 98304, L_BIAS = 157312, L_RS = 159360, L_MSK = 159872, L_DUMP = 161920, L_FLG = 162176, L_TOTAL = 163840;
constexpr u32 SLOT_A = 24576, SLOT_B = 18432, L_RES = L_KV + 43008;
constexpr u32 SENT = 0xffffffffu;
constexpr u32 CODE_PF = 26624;

DEV u32x4 make_srd(const void* p, u32 bytes) { u64 a = (u64)p; return u32x4{(u32)a, (u32)(a >> 32) & 0xffffu, bytes, 0x00020000u}; }
template <int LI, int SI> DEV void dma16(u32x4 srd, u32 voff, u32 soff, u32 lds) {
  u32 t;
  asm volatile("s_add_u32 m0, %1, %5\n\ts_add_u32 %0, %4, %6\n\tbuffer_load_dwordx4 %2, %3, %0 offen lds"
               : "=&s"(t) : "s"(lds), "v"(voff), "s"(srd), "s"(soff), "i"(LI), "i"(SI) : "memory", "scc");
}
template <int LI> DEV void dma16s(u32x4 srd, u32 voff, u32& so, u32 inc, u32 lds) {
  asm volatile("s_add_u32 m0, %1, %5\n\ts_add_u32 %0, %0, %4\n\tbuffer_load_dwordx4 %2, %3, %0 offen sc1 lds"
               : "+s"(so) : "s"(lds), "v"(voff), "s"(srd), "s"(inc), "i"(LI) : "memory", "scc");
}
DEV void touch(u32x4 srd, u32 voff, u32 lds) {
  asm volatile("s_mov_b32 m0, %0\n\ts_nop 0\n\tbuffer_load_dword %1, %2, 0 offen lds" :: "s"(lds), "v"(voff), "s"(srd) : "memory");
}
DEV void fetch_flags(u32x4 srd, u32 voff, u32 lds) {
  asm volatile("s_mov_b32 m0, %0\n\ts_mov_b64 exec, 0xffff\n\tbuffer_load_dword %1, %2, 0 offen sc1 lds\n\ts_mov_b64 exec, -1" :: "s"(lds), "v"(voff), "s"(srd) : "memory");
}
template <int LI, int SI> DEV void dma16m(u32x4 srd, u32 voff, u32 soff, u32 lds, u64 mask) {
  u32 t;
  asm volatile("s_add_u32 m0, %1, %6\n\ts_add_u32 %0, %4, %7\n\ts_mov_b64 exec, %5\n\tbuffer_load_dwordx4 %2, %3, %0 offen lds\n\ts_mov_b64 exec, -1"
               : "=&s"(t) : "s"(lds), "v"(voff), "s"(srd), "s"(soff), "s"(mask), "i"(LI), "i"(SI) : "memory", "scc");
}
template <int OFF> DEV u32x4 ld16(u32x4 srd, u32 voff, u32 soff) {
  u32x4 r; asm volatile("buffer_load_dwordx4 %0, %1, %2, %3 offen offset:%4" : "=v"(r) : "v"(voff), "s"(srd), "s"(soff), "i"(OFF) : "memory"); return r;
}
template <int OFF> DEV u32x2 ld8c(u32x4 srd, u32 voff, u32 soff) {
  u32x2 r; asm volatile("buffer_load_dwordx2 %0, %1, %2, %3 offen offset:%4 sc1" : "=v"(r) : "v"(voff), "s"(srd), "s"(soff), "i"(OFF) : "memory"); return r;
}
template <int OFF> DEV void st16c(u32x4 v, u32x4 srd, u32 voff, u32 soff) {
  asm volatile("buffer_store_dwordx4 %0, %1, %2, %3 offen offset:%4 sc1\n\ts_nop 1" :: "v"(v), "v"(voff), "s"(srd), "s"(soff), "i"(OFF) : "memory");
}
template <int OFF> DEV void st8c(u32x2 v, u32x4 srd, u32 voff, u32 soff) {
  asm volatile("buffer_store_dwordx2 %0, %1, %2, %3 offen offset:%4 sc1\n\ts_nop 1" :: "v"(v), "v"(voff), "s"(srd), "s"(soff), "i"(OFF) : "memory");
}
DEV void st4c(u32 v, u32x4 srd, u32 voff, u32 soff) {
  asm volatile("buffer_store_dword %0, %1, %2, %3 offen sc1\n\ts_nop 1" :: "v"(v), "v"(voff), "s"(srd), "s"(soff) : "memory");
}
template <int OFF> DEV void st8(u32x2 v, u32x4 srd, u32 voff, u32 soff) {
  asm volatile("buffer_store_dwordx2 %0, %1, %2, %3 offen offset:%4\n\ts_nop 1" :: "v"(v), "v"(voff), "s"(srd), "s"(soff), "i"(OFF) : "memory");
}
template <class T> DEV void pins(T& v) { asm volatile("" : "+s"(v)); }
DEV const char* arg(int i) {
  const u64 __attribute__((address_space(4)))* ka = (const u64 __attribute__((address_space(4)))*)__builtin_amdgcn_kernarg_segment_ptr();
  asm volatile("" : "+s"(ka));
  return (const char*)ka[i];
}
template <class T> DEV void pin(T& v) { asm volatile("" : "+v"(v)); }
#define STAMP(i)
#define STAMPR(i)
template <int N> DEV void wait_vm() { asm volatile("s_waitcnt vmcnt(%0)" :: "i"(N) : "memory"); }
DEV void wait_lgkm0() { asm volatile("s_waitcnt lgkmcnt(0)" ::: "memory"); }
DEV void fence_sched() { __builtin_amdgcn_sched_barrier(0); }
DEV void bar() { fence_sched(); __builtin_amdgcn_s_barrier(); fence_sched(); }

DEV float u2f(u32 v) { return __builtin_bit_cast(float, v); }
DEV u32 f2u(float v) { return __builtin_bit_cast(u32, v); }
DEV bf16x8 as_bf(u32x4 v) { return __builtin_bit_cast(bf16x8, v); }
DEV u32 pk2(float a, float b) { return __builtin_bit_cast(u32, __builtin_convertvector(f32x2{a, b}, bf16x2)); }
DEV float blo(u32 d) { return u2f(d << 16); }
DEV float bhi(u32 d) { return u2f(d & 0xffff0000u); }
DEV float rbf(float f) { return blo(pk2(f, f)); }
DEV float max3(float a, float b, float c) { return __builtin_elementwise_maximum(__builtin_elementwise_maximum(a, b), c); }
DEV u32 umax(u32 a, u32 b) { return a > b ? a : b; }
DEV u32 umin(u32 a, u32 b) { return a < b ? a : b; }
DEV bool all_lanes(bool p) { return __builtin_amdgcn_ballot_w64(!p) == 0; }
DEV float dot2(u32 a, u32 b, float c) { return __builtin_amdgcn_fdot2_f32_bf16(__builtin_bit_cast(bf16x2, a), __builtin_bit_cast(bf16x2, b), c, false); }
DEV float quad_sum(float v) {
  v += u2f(__builtin_amdgcn_mov_dpp(f2u(v), 0xb1, 0xf, 0xf, true));
  return v + u2f(__builtin_amdgcn_mov_dpp(f2u(v), 0x4e, 0xf, 0xf, true));
}
DEV u32x2 lds_r8(u32 a) { return *(volatile LAS u32x2*)a; }
DEV u32x4 cat(u32x2 a, u32x2 b) { return u32x4{a[0], a[1], b[0], b[1]}; }
DEV u32x4 lds_r16(u32 a) { return *(LAS u32x4*)a; }
DEV void lds_w16(u32 a, u32x4 v) { *(LAS u32x4*)a = v; }
DEV bf16x8 tr_frag(u32 a0, u32 a1) {
  s16x4 lo = __builtin_amdgcn_ds_read_tr16_b64_v4i16((LAS s16x4*)a0);
  s16x4 hi = __builtin_amdgcn_ds_read_tr16_b64_v4i16((LAS s16x4*)a1);
  return __builtin_bit_cast(bf16x8, __builtin_shufflevector(lo, hi, 0, 1, 2, 3, 4, 5, 6, 7));
}
template <int I> struct IC { static constexpr int v = I; };
template <int I, int N, class F> DEV void sfor(F&& f) { if constexpr (I < N) { f(IC<I>{}); sfor<I + 1, N>(f); } }
#define SPINS (1u << 18)
#define RARE(x) __builtin_expect(!!(x), 0)
#define MFMA32(a, b, c) __builtin_amdgcn_mfma_f32_32x32x16_bf16((a), (b), (c), 0, 0, 0)
DEV void mfma16_acc(f32x4& c, bf16x8 a, u32x4 b) { asm volatile("v_mfma_f32_16x16x32_bf16 %0, %1, %2, %0" : "+v"(c) : "v"(a), "v"(b)); }
#define MFMA16(a, b, c) __builtin_amdgcn_mfma_f32_16x16x32_bf16((a), (b), (c), 0, 0, 0)

DEV float gelu_a(float x) { return __builtin_amdgcn_exp2f(x * __builtin_fmaf(x * x, -0.10294324f, -2.3022082f)); }
DEV float gelu_b(float x, float e) { return x * __builtin_amdgcn_rcpf(1.f + e); }
DEV void poll_words(const char* ws, u32 off, bool active, u64 tag) {
#pragma unroll 1
  for (u32 spins = 0; spins < SPINS; ++spins) {
    u64 v = active ? __hip_atomic_load((GAS u64*)(ws + off), RLX_AGENT) : tag;
    if (all_lanes(v == tag)) break;
    __builtin_amdgcn_s_sleep(2);
  }
  __builtin_amdgcn_fence(__ATOMIC_ACQUIRE, "wavefront");
}
DEV int bid_of(int m, int kq, int s) { return ((m & 7) * 4 + s) * 8 + (m >> 3) * 4 + kq; }
constexpr int line_of(int s, int n) {
  const int LG[4] = {0, 4, 8, 14};
  return n < 4 ? LG[(s + n) & 3] : n < 6 ? n + 8 : n < 18 ? LG[(s + n / 3 - 2) & 3] + 1 + n % 3 : n < 20 ? 31 : n - 2;
}
constexpr u64 line_tab(int s, int half) { u64 t = 0; for (int i = 0; i < 12; ++i) t |= (u64)line_of(s, 12 * half + i) << (5 * i); return t; }
constexpr u64 diag_mask(int e) {
  u64 mk = 0;
  for (int l = 0; l < 64; ++l) if ((e & 3) + 8 * (e >> 2) + 4 * (l >> 5) <= (l & 31)) mk |= 1ull << l;
  return mk;
}

__global__ __launch_bounds__(512, 2) void fused_block(const u32* __restrict__ x, const u32* __restrict__ wqkv, const u32* __restrict__ wo,
                                                      const u32* __restrict__ w1, const u32* __restrict__ b1, const u32* __restrict__ w2,
                                                      const u32* __restrict__ b2, u32* __restrict__ out, char* __restrict__ ws) {
  __shared__ __attribute__((aligned(1024))) char smem[L_TOTAL];
  const u32 LB = (u32)(size_t)(LAS char*)smem;
  const int tid = __builtin_amdgcn_workitem_id_x(), l = tid & 63;
  const int w = __builtin_amdgcn_readfirstlane(tid >> 6) & 7, r = w & 3, c = w >> 2;
  const int bid = __builtin_amdgcn_workgroup_id_x(), kq = bid & 3, mh = (bid >> 2) & 1, m = 8 * mh + (bid >> 5), s = (bid >> 3) & 3;
  STAMPR(62); STAMP(0);
  const int h = l >> 5, t16 = l & 15, g4 = l >> 4, q4 = (l & 15) >> 2, p4 = l & 3, blk = (l >> 4) & 1;
  const u32 xso = (128 * m + 32 * r + 16 * c) * 1152;
  const u32x4 sWS = make_srd(ws, WS_END);
  u32x4 xr[18];
  u32x2 ep; u32 braw;
  {
    const u32x4 sX = make_srd(x, 2048 * 1152);
    const u32 vo = (l >> 2) * 1152 + p4 * 16;
#define XL(i) xr[i] = ld16<64 * (i)>(sX, vo, xso);
    XL(0) XL(1) XL(2) XL(3) XL(4) XL(5) XL(6) XL(7) XL(8) XL(9) XL(10) XL(11) XL(12) XL(13) XL(14) XL(15) XL(16) XL(17)
#undef XL
    asm volatile("buffer_load_dwordx2 %0, %1, %2, 0 offen sc1" : "=v"(ep) : "v"(WS_EPOCH), "s"(sWS) : "memory");
    asm volatile("buffer_load_dword %0, %1, %2, 0 offen" : "=v"(braw) : "v"(tid < 192 ? 2304 * kq + 512 * s + 4 * tid : OOB), "s"(make_srd(b1, 9216)) : "memory");
    u64 pc; asm volatile("s_getpc_b64 %0" : "=s"(pc));
    touch(make_srd((const void*)(pc & ~127ull), CODE_PF), l < 1 ? ((bid >> 3) * 8 + w) * 128 : OOB, LB + L_DUMP);
    touch(w == 7 ? make_srd(out, 2048 * 1152) : sWS, w == 7 ? (l < 2 ? ((bid >> 3) * 2 + l) * 36864 : OOB) : (l < 3 ? 65536 + (((bid >> 3) * 7 + w) * 3 + l) * 65536 : OOB), LB + L_DUMP);
  }
  STAMP(21);
  const bool isq = s < 3;
  const int NC = isq ? 7 : 6, NQ = isq ? 3 : 0;
  const int ql0 = 2 * kq + (kq < 2 ? 2 : 0), ql1 = 2 * kq + (kq < 2 ? 0 : 1);
  const u32x4 sW1 = make_srd(w1, 576 * 9216);
  const u32 pcs = 16 * ((l & 7) ^ (4 * blk));
  const u32 vo_f = (l >> 3) * 9216 + pcs;
  const u32 dl = LB + w * 3072;
  const u32 fcol = 2304 * kq + 512 * s;
  u32x4 sA = isq ? make_srd(wqkv, 576 * 3456) : sW1;
  u32 voA = isq ? ((l >> 3) * 3456 + pcs) ^ (kq < 2 ? 0 : 64) : vo_f, i8 = isq ? 8 * 3456 : 8 * 9216, i176 = 22 * i8;
  u32 so = 3 * w * i8 + (isq ? 1152 * s + 128 * ql0 : fcol) - i176;
#define DMA_A(i, lds) dma16s<(i) * 1024>(sA, voA, so, (i) == 0 ? i176 : i8, lds)
  if (w == 0) {
    u32x4 mk[2];
#pragma unroll
    for (int e = 0; e < 8; ++e) { const int d = (l & 31) - 4 * h - ((2 * e & 3) + 8 * (e >> 1)); mk[e >> 2][e & 3] = d < 0 ? 0u : (d == 0 ? 0xffffu : ~0u); }
    lds_w16(LB + L_MSK + l * 16, mk[0]); lds_w16(LB + L_MSK + 1024 + l * 16, mk[1]);
  }
  u32 tv0, tv1;
  {
    const int wi = (m & 7) * 8 + w, trow = wi * 9 + l % 9, ln = l / 9;
    tv0 = ln < 2 ? trow * (i8 >> 3) + (isq ? 1152 * s + 128 * (ql1 + ln) : fcol + 128 * (1 + ln)) : (l < 21 ? (384 + wi * 3 + l - 18) * (i8 >> 3) + (isq ? 1152 * s + 128 * ql0 : fcol) : OOB);
    tv1 = ln < (isq ? 4 : 3) ? trow * 9216 + fcol + 128 * (ln + (isq ? 0 : 3)) : OOB;
    pin(tv0); pin(tv1);
  }
  u64 tag;
  u32x4 xf[36];
  float rs;
  const u32x4 sG = make_srd(ws + WS_G + (kq * 16 + m) * G_SZ, G_SZ);
  f32x16 prev;
  u32 ba = LB + L_BIAS + c * 128 + h * 16;
  u32 hso = (8 * s + c) * 8192, gvo;
  {
    const u32 wl = LB + h * 1024 + q4 * 128 + 64 * (c ^ ((q4 >> 1) & 1)) + 32 * blk + 8 * p4;
    constexpr int PF = 4;
    bf16x8 wf[PF];
    f32x16 acc;
    {
      u32 xwa = LB + p4 * 512 + (((16 * c + (l >> 2)) ^ (p4 << 1)) << 4), xra = LB + h * 512 + (((l & 31) ^ (h << 1)) << 4);
      u32 xwb = xwa + L_KV + r * 12288, xrc = xra + L_KV + r * 12288, xrd = xrc ^ 64;
      xwa += 2 * SLOT_A + r * 24576; xra += 2 * SLOT_A + r * 24576;
      u32 xrb = xra ^ 64;
      pin(xwa); pin(xra); pin(xrb);
      float s0 = 0.f, s1 = 0.f, q0 = 0.f, q1 = 0.f;
      auto sq = [&](int i) { q0 = dot2(xr[i][0], xr[i][0], q0); q1 = dot2(xr[i][1], xr[i][1], q1); q0 = dot2(xr[i][2], xr[i][2], q0); q1 = dot2(xr[i][3], xr[i][3], q1); };
#pragma unroll
      for (int T = 0; T < 6; ++T) {
        if (T == 0) wait_vm<19>(); else if (T == 1) wait_vm<16>(); else if (T == 2) wait_vm<13>(); else if (T == 3) wait_vm<10>(); else if (T == 4) wait_vm<7>(); else wait_vm<4>();
#pragma unroll
        for (int i = 3 * T; i < 3 * T + 3; ++i) {
          pin(xr[i]);
          s0 = dot2(xr[i][0], 0x3f803f80u, s0); s1 = dot2(xr[i][1], 0x3f803f80u, s1); s0 = dot2(xr[i][2], 0x3f803f80u, s0); s1 = dot2(xr[i][3], 0x3f803f80u, s1);
        }
        fence_sched();
      }
      STAMP(1);
      const float mean = quad_sum(s0 + s1) * (1.f / 576.f), nmean = -rbf(mean);
      auto centre = [&](int i) {
        float d0, d1, d2, d3, d4, d5, d6, d7; u32 o0, o1, o2, o3;
        asm volatile("s_nop 1\n\tv_dot2_f32_bf16 %4, %12, %17, %16\n\tv_dot2_f32_bf16 %5, %12, %18, %16\n\tv_dot2_f32_bf16 %6, %13, %17, %16\n\tv_dot2_f32_bf16 %7, %13, %18, %16\n\t"
                     "v_dot2_f32_bf16 %8, %14, %17, %16\n\tv_dot2_f32_bf16 %9, %14, %18, %16\n\tv_dot2_f32_bf16 %10, %15, %17, %16\n\tv_dot2_f32_bf16 %11, %15, %18, %16\n\t"
                     "v_cvt_pk_bf16_f32 %0, %4, %5\n\tv_cvt_pk_bf16_f32 %1, %6, %7\n\tv_cvt_pk_bf16_f32 %2, %8, %9\n\tv_cvt_pk_bf16_f32 %3, %10, %11"
                     : "=v"(o0), "=v"(o1), "=v"(o2), "=v"(o3), "=&v"(d0), "=&v"(d1), "=&v"(d2), "=&v"(d3), "=&v"(d4), "=&v"(d5), "=&v"(d6), "=&v"(d7)
                     : "v"(xr[i][0]), "v"(xr[i][1]), "v"(xr[i][2]), "v"(xr[i][3]), "v"(nmean), "s"(0x00003f80u), "s"(0x3f800000u));
        xr[i] = u32x4{o0, o1, o2, o3};
      };
#pragma unroll
      for (int i = 0; i < 12; ++i) {
        if (i < 6) { if (i % 3 == 0) DMA_A(0, dl + (i / 3) * SLOT_A); else if (i % 3 == 1) DMA_A(1, dl + (i / 3) * SLOT_A); else DMA_A(2, dl + (i / 3) * SLOT_A); }
        if (i == 6) touch(sA, tv0, LB + L_DUMP);
        centre(i); lds_w16(xwa + i * 2048, xr[i]);
      }
      wait_lgkm0(); bar();
      STAMP(23);
#pragma unroll
      for (int i = 0; i < 6; ++i) {
#pragma unroll
        for (int j = 4 * i; j < 4 * i + 4; ++j) xf[j] = lds_r16((j & 1 ? xrb : xra) + j * 1024);
        centre(12 + i); sq(2 * i); sq(2 * i + 1); sq(12 + i);
        fence_sched();
      }
      const float dm = mean + nmean;
      const float var = __builtin_fmaxf(quad_sum(q0 + q1) * (1.f / 576.f) - dm * dm, 0.f);
      const float rsd = __builtin_amdgcn_rcpf(rbf(__builtin_amdgcn_sqrtf(rbf(rbf(var) + 1e-5f))));
      if (p4 == 0) *(LAS float*)(LB + L_RS + (32 * r + 16 * c + (l >> 2)) * 4) = rsd;
      wait_vm<1>();
      pin(ep); pin(braw);
      if (tid < 256) *(LAS f32x2*)(LB + L_BIAS + tid * 8) = f32x2{blo(braw), bhi(braw)};
      tag = (((u64)(u32)__builtin_amdgcn_readfirstlane(ep[1]) << 32) | (u32)__builtin_amdgcn_readfirstlane(ep[0])) + 1;
      pin(xwb); pin(xrc); pin(xrd);
      wait_lgkm0(); bar();
      STAMP(2);
      const bool act = !(isq && c);
#pragma unroll
      for (int j = 0; j < PF; ++j) wf[j] = tr_frag(wl + j * 2048, wl + j * 2048 + 512);
      DMA_A(0, dl + 2 * SLOT_A); DMA_A(1, dl + 2 * SLOT_A); DMA_A(2, dl + 2 * SLOT_A);
      if (isq) { voA = (l >> 3) * 3456 + pcs; so += 128 * (ql1 - ql0) - 576 * 3456; } else so += 128 - 576 * 9216;
#pragma unroll
      for (int i = 0; i < 6; ++i) lds_w16(xwb + i * 2048, xr[12 + i]);
      rs = *(LAS float*)(LB + L_RS + (32 * r + (l & 31)) * 4);
      sfor<0, 9>([&](auto B_) __attribute__((always_inline)) {
        constexpr int kb4 = decltype(B_)::v;
        if (act) {
          sfor<4 * kb4, 4 * kb4 + 4>([&](auto KK) __attribute__((always_inline)) {
            constexpr int kk = decltype(KK)::v;
            if constexpr (kk == 0) acc = MFMA32(wf[0], as_bf(xf[0]), f32x16{});
            else if constexpr (kk < 35) acc = MFMA32(wf[kk % PF], as_bf(xf[kk]), acc);
            else prev = MFMA32(wf[kk % PF], as_bf(xf[kk]), acc);
            if constexpr (kk < 35) pin(acc); else pin(prev);
            fence_sched();
            if constexpr (kk < 32) { const u32 pa = wl + ((kk + PF) / 12) * SLOT_A + ((kk + PF) % 12) * 2048; wf[kk % PF] = tr_frag(pa, pa + 512); }
            fence_sched();
          });
        }
        DMA_A(kb4 % 3, dl + ((kb4 / 3 + 3) & 3) * SLOT_A);
        if constexpr (kb4 >= 3 && kb4 < 6) {
#pragma unroll
          for (int j = 4 * (kb4 - 3); j < 4 * (kb4 - 3) + 4; ++j) xf[24 + j] = lds_r16((j & 1 ? xrd : xrc) + j * 1024);
        }
        if constexpr (kb4 == 2) { asm volatile("s_waitcnt lgkmcnt(8)" ::: "memory"); if (!act) wait_lgkm0(); bar(); }
        if constexpr (kb4 == 4) { wait_vm<5>(); bar(); }
        if constexpr (kb4 == 5) { bar(); }
        if constexpr (kb4 == 8) {
          touch(sW1, tv1, LB + L_DUMP);
          so += 128 - 72 * i8;
          wait_vm<4>(); bar();
#pragma unroll
          for (int j = 0; j < PF; ++j) wf[j] = tr_frag(wl + 3 * SLOT_A + j * 2048, wl + 3 * SLOT_A + j * 2048 + 512);
        }
        fence_sched();
      });
    }
    STAMP(3);
    u32 R0 = 3 * SLOT_A, R1 = 0, R2 = SLOT_A, R3 = 2 * SLOT_A;
    u32 D0 = dl + R3, D1 = dl + R0, D2 = dl + R1;
    auto kstep = [&](auto KK) __attribute__((always_inline)) {
      constexpr int kk = decltype(KK)::v, sl = kk / 12, ks = kk % 12;
      if constexpr (kk == 0) acc = MFMA32(wf[0], as_bf(xf[0]), f32x16{});
      else if constexpr (kk < 35) acc = MFMA32(wf[ks % PF], as_bf(xf[kk]), acc);
      else asm volatile("v_mfma_f32_32x32x16_bf16 %0, %1, %2, %3" : "=&v"(prev) : "v"(wf[ks % PF]), "v"(xf[kk]), "v"(acc));
      if constexpr (kk < 35) pin(acc);
      fence_sched();
      constexpr int sn = (kk + PF) / 12;
      const u32 pa = wl + (sn == 0 ? R0 : sn == 1 ? R1 : sn == 2 ? R2 : R3) + ((ks + PF) % 12) * 2048;
      wf[ks % PF] = tr_frag(pa, pa + 512);
      fence_sched();
      const u32 dst = sl == 0 ? D0 : sl == 1 ? D1 : D2;
      if constexpr (ks == 1) DMA_A(0, dst);
      if constexpr (ks == 5) DMA_A(1, dst);
      if constexpr (ks == 9) DMA_A(2, dst);
      if constexpr (kk == 34) { D2 = D1; D1 = D0; D0 = dl + R2; }
      if constexpr (kk == 35) { const u32 t = R3; R3 = R2; R2 = R1; R1 = R0; R0 = t; }
    };
    if (isq) {
      const u32 qvo = h * 1024 + (32 * r + (l & 31)) * 8;
      const u32x4 sQ = make_srd(ws + WS_QKV + (kq * 16 + m) * QKV_SZ + s * 36864, 36864);
      int oq = c ? -65536 : (64 * ql0 + (kq < 2 ? 0 : 32) - 144 * kq) * 256, oqn = (64 * ql1 + 32 * c - 144 * kq) * 256;
      const u32x4 sW2 = make_srd(arg(5) + 1152 * kq * 1152, 1152 * 1152);
      u32 tv2 = l < 18 ? ((((m & 7) * 3 + s) * 8 + w) * 54 + l) * 128 : OOB;
      const float re = s == 0 ? rs * 0.34004557f : rs;
#pragma unroll 1
      for (int ci = 1; ci <= 3; ++ci) {
        u32x2 un;
        sfor<0, 36>([&](auto KK) __attribute__((always_inline)) {
          constexpr int kk = decltype(KK)::v;
          if constexpr (kk == 33) { if (RARE(ci == 3)) wait_vm<0>(); }
          kstep(KK);
          if constexpr (kk == 2 || kk == 3 || kk == 6 || kk == 7 || kk == 14 || kk == 15 || kk == 18 || kk == 19) {
            constexpr int e = (kk & 1) + (kk >= 6 ? 2 : 0) + (kk >= 14 ? 2 : 0) + (kk >= 18 ? 2 : 0);
            u32 v = pk2(prev[2 * e] * re, prev[2 * e + 1] * re); pin(v); un[e & 1] = v;
          }
          if constexpr (kk == 4 || kk == 10 || kk == 16 || kk == 22) {
            const int o = kk < 12 ? oq : oq + 4096;
            if constexpr (kk == 4 || kk == 16) st8c<0>(un, sQ, qvo, o < 0 ? OOB : (u32)o); else st8c<2048>(un, sQ, qvo, o < 0 ? OOB : (u32)o);
          }
          if constexpr (kk == 34) {
            touch(sW2, tv2, LB + L_DUMP); tv2 += 18 * 128;
            oq = oqn; oqn += 16384;
            so += 128 - 72 * i8;
            if (RARE(ci == 1)) { sA = sW1; voA = vo_f; i8 = 8 * 9216; i176 = 176 * 9216; so = 24 * w * 9216 + fcol - 176 * 9216; }
          }
          fence_sched();
          if constexpr (kk % 12 == 11) { if constexpr (kk == 11) wait_vm<6>(); else if constexpr (kk == 23) wait_vm<6>(); else wait_vm<5>(); bar(); }
        });
      }
    }
    STAMP(4);
    if (tid == 0) __hip_atomic_store((GAS u64*)(ws + WS_FLAG + bid * 128), tag, RLX_AGENT);
    gvo = 2 * r * 1024 + blk * 1024 + h * 256 + t16 * 16;
    f32x4 bq = *(LAS f32x4*)ba;
#pragma unroll 1
    for (int left = NC - NQ - 2;; --left) {
      u32x4 un[2];
      float xa, xb, ea, eb;
      sfor<0, 36>([&](auto KK) __attribute__((always_inline)) {
        constexpr int kk = decltype(KK)::v;
        if constexpr (kk == 33) { if (RARE(left == 1)) wait_vm<0>(); }
        kstep(KK);
        if constexpr (kk >= 2 && kk < 34) {
          constexpr int t = kk - 2, e = 2 * (t >> 2);
          if constexpr ((t & 3) == 0) { xa = __builtin_fmaf(prev[e], rs, bq[e & 3]); ea = gelu_a(xa); pin(ea); }
          if constexpr ((t & 3) == 1) { xb = __builtin_fmaf(prev[e + 1], rs, bq[(e & 3) + 1]); eb = gelu_a(xb); pin(eb); }
          if constexpr ((t & 7) == 5) { if constexpr (t == 29) ba += 256; bq = *(LAS f32x4*)(ba + (((t >> 3) + 1) & 3) * 32); }
          if constexpr ((t & 3) == 2) { ea = gelu_b(xa, ea); pin(ea); }
          if constexpr ((t & 3) == 3) { u32 v = pk2(ea, gelu_b(xb, eb)); pin(v); un[t >> 4][(t >> 2) & 3] = v; }
        }
        if constexpr (kk == 0) {
          if (RARE(left == 0)) { int b_ = bid; pins(b_); if (tid == 0) __hip_atomic_store((GAS u64*)(ws + WS_FLAG + (256 + b_) * 128), tag, RLX_AGENT); }
          if (RARE(left == 1)) {
            int m_ = m, w_ = w, t_ = tid; pins(m_); pins(w_); pin(t_);
            const int l_ = (t_ >> 1) & 7, pm = l_ < 3 ? m_ : (l_ < 5 ? m_ - 1 : m_ - 2);
            fetch_flags(sWS, WS_FLAG + bid_of(pm < 0 ? m_ : pm, kq, l_ < 3 ? l_ : 1 + (l_ & 1)) * 128 + (t_ & 1) * 4, LB + L_FLG + w_ * 64);
          }
        }
        if constexpr (kk == 22) st16c<0>(un[0], sG, gvo, hso);
        if constexpr (kk == 34) {
          st16c<512>(un[1], sG, gvo, hso);
          hso += 16384;
          so += 128 - 576 * 9216;
          if (RARE(left == 1)) {
            int m_ = m, w_ = w, t_ = tid; pins(m_); pins(w_); pin(t_);
            const u32x2 fl = lds_r8(LB + L_FLG + w_ * 64 + (t_ & 7) * 8);
            const int l_ = t_ & 7, pm = l_ < 3 ? m_ : (l_ < 5 ? m_ - 1 : m_ - 2);
            if (!all_lanes(fl[0] == (u32)tag && fl[1] == (u32)(tag >> 32))) poll_words(ws, WS_FLAG + bid_of(pm < 0 ? m_ : pm, kq, l_ < 3 ? l_ : 1 + (l_ & 1)) * 128, true, tag);
            sA = make_srd(ws + WS_QKV + kq * 16 * QKV_SZ, w_ < 6 ? 16 * QKV_SZ : 0);
            voA = (t_ & 63) * 16; i8 = QKV_SZ; i176 = 1024 - 2 * QKV_SZ;
            so = (m_ - 2) * QKV_SZ + (w_ < 3 ? 36 + 3 * w_ : 63 + 3 * w_) * 1024 + 9216 * s - i176;
            D0 = w_ < 6 ? LB + L_KV + 3 * w_ * PP : L_TOTAL; D1 = D0 + PP; D2 = D1 + PP;
            if (w_ < 6) *(LAS u32x2*)(LB + L_KV + 18 * PP + t_ * 8) = u32x2{t_ < 256 - 128 * m_ ? 0xff7f3f80u : 0x00003f80u, 0x3f803f80u};
          }
        }
        fence_sched();
        if constexpr (kk == 11) { wait_vm<4>(); bar(); }
        if constexpr (kk == 23) {
          if (left) { wait_vm<4>(); bar(); }
          else { int w_ = w, b_ = bid, t_ = tid; pins(w_); pins(b_); pin(t_); fetch_flags(sWS, WS_FLAG + (256 + (b_ & ~24)) * 128 + ((t_ >> 1) & 3) * 1024 + (t_ & 1) * 4, LB + L_FLG + w_ * 64); }
        }
      });
      if (left == 0) break;
      wait_vm<5>(); bar();
    }
  }
  STAMP(10);
  bar();
  const auto rWS = __builtin_amdgcn_make_buffer_rsrc(ws, 0, WS_END, 0x00020000);
  const u64 LT0 = s == 0 ? line_tab(0, 0) : s == 1 ? line_tab(1, 0) : s == 2 ? line_tab(2, 0) : line_tab(3, 0);
  const u64 LT1 = s == 0 ? line_tab(0, 1) : s == 1 ? line_tab(1, 1) : s == 2 ? line_tab(2, 1) : line_tab(3, 1);
  u32 ltab = ((u32)(l < 12 ? LT0 >> (5 * l) : LT1 >> (5 * l - 60)) & 31) * (c ? 73728 : 16384);
  pin(ltab);
  auto line = [&](int n) -> u32 { return __builtin_amdgcn_readlane(ltab, n); };
  u32x4 un[2];
  auto last_gelu = [&](auto&& between) __attribute__((always_inline)) {
    asm volatile("s_nop 7\n\ts_nop 3" ::: "memory");
    sfor<0, 4>([&](auto G_) __attribute__((always_inline)) {
      constexpr int gq = decltype(G_)::v;
      between(G_);
      const f32x4 b = *(LAS f32x4*)(ba + gq * 32);
      float y[4];
#pragma unroll
      for (int e = 0; e < 4; ++e) { const float xx = __builtin_fmaf(prev[4 * gq + e], rs, b[e]); y[e] = gelu_b(xx, gelu_a(xx)); }
      u32 u0 = pk2(y[0], y[1]), u1 = pk2(y[2], y[3]); pin(u0); pin(u1);
      un[gq >> 1][2 * (gq & 1)] = u0; un[gq >> 1][2 * (gq & 1) + 1] = u1;
      fence_sched();
    });
    wait_vm<8>(); wait_lgkm0(); bar();
    st16c<0>(un[0], sG, gvo, hso); st16c<512>(un[1], sG, gvo, hso);
  };
  f32x4 v0, v1, v8;
  if (c == 0) {
    const auto rG = __builtin_amdgcn_make_buffer_rsrc(ws + WS_G + (kq * 16 + m) * G_SZ, 0, G_SZ, 0x00020000);
    const u32 gl0 = 2 * r * 1024 + l * 16, gl1 = gl0 + 8192;
    u32x4 gf[6][4];
#define ISSUE_G(sl_, i, off) gf[sl_][i] = __builtin_amdgcn_raw_buffer_load_b128(rG, ((i) & 2 ? gl1 : gl0) + ((i) & 1) * 1024, off, 16)
    u32 wb = LB + (16 * (g4 >> 1) + 4 * (g4 & 1) + q4) * 288 + 8 * p4;
    u32 wo1 = LB + L_KV + (20 * (g4 >> 1) + 4 * (g4 & 1) + q4) * 288 + 8 * p4, wo2 = LB + L_KV + (36 * (g4 >> 1) + 2 * (g4 & 1) + 16 + q4) * 288 + 8 * p4;
    pin(wb); pin(wo1); pin(wo2);
    f32x4 ac[2][9];
#pragma unroll
    for (int d = 0; d < 9; ++d) { ac[0][d] = f32x4{}; ac[1][d] = f32x4{}; pin(ac[0][d]); pin(ac[1][d]); }
    { const u32 go = line(0); ISSUE_G(0, 0, go); ISSUE_G(0, 1, go); ISSUE_G(0, 2, go); ISSUE_G(0, 3, go); }
    wait_vm<8>();
    { const u32x2 fl = lds_r8(LB + L_FLG + w * 64 + (l & 3) * 8);
      if (RARE(!all_lanes(fl[0] == (u32)tag && fl[1] == (u32)(tag >> 32)))) poll_words(ws, WS_FLAG + (256 + (bid & ~24)) * 128 + (l & 3) * 1024, l < 4, tag); }
    last_gelu([&](auto G_) __attribute__((always_inline)) {
      constexpr int g = decltype(G_)::v + 1;
      if constexpr (g < 4) { const u32 go = line(g); ISSUE_G(g, 0, go); ISSUE_G(g, 1, go); ISSUE_G(g, 2, go); ISSUE_G(g, 3, go); }
    });
    STAMP(11);
    bf16x8 af[9];
#pragma unroll
    for (int d = 0; d < 8; ++d) af[d] = tr_frag(wb + d * 32, wb + d * 32 + 2304);
    auto step = [&](auto Q_, const u32x4& b0, const u32x4& b1, u32 pa, auto PRE) __attribute__((always_inline)) {
      constexpr int d = decltype(Q_)::v % 9, dn = (d + 8) % 9;
      s16x4 lo, hi;
      mfma16_acc(ac[0][d], af[d], b0); fence_sched();
      if constexpr (decltype(PRE)::v) lo = __builtin_amdgcn_ds_read_tr16_b64_v4i16((LAS s16x4*)pa);
      fence_sched();
      mfma16_acc(ac[1][d], af[d], b1); fence_sched();
      if constexpr (decltype(PRE)::v) { hi = __builtin_amdgcn_ds_read_tr16_b64_v4i16((LAS s16x4*)(pa + 2304)); af[dn] = __builtin_bit_cast(bf16x8, __builtin_shufflevector(lo, hi, 0, 1, 2, 3, 4, 5, 6, 7)); }
      fence_sched();
    };
    u32 cur = wb, rbo = 0;
#pragma unroll 1
    for (int n0 = 0; n0 < 18; n0 += 6) {
      sfor<0, 6>([&](auto B_) __attribute__((always_inline)) {
        constexpr int b = decltype(B_)::v;
        u32 nxt, go;
        sfor<0, 18>([&](auto Q_) __attribute__((always_inline)) {
          constexpr int q = decltype(Q_)::v, qn = q + 8;
          if constexpr (q == 9) { rbo = rbo == 4 * SLOT_B ? 0 : rbo + SLOT_B; nxt = (b == 5 && n0 == 12) ? wo1 : wb + rbo; pin(nxt); }
          step(Q_, gf[b][2 * (q / 9)], gf[b][2 * (q / 9) + 1], qn < 18 ? cur + (qn / 9) * 9216 + (qn % 9) * 32 : nxt + (qn - 18) * 32, IC<1>{});
          if constexpr (q == 0) go = line(n0 + b + 4);
          if constexpr (q == 1 || q == 5 || q == 9 || q == 13) ISSUE_G((b + 4) % 6, (q - 1) / 4, go);
          fence_sched();
        });
        cur = nxt;
        if constexpr (b == 1) { if (RARE(n0 == 0)) wait_vm<0>(); }
        bar();
      });
    }
    STAMP(15);
    const u32 pbase = WS_P + r * 1024;
    sfor<0, 54>([&](auto Q_) __attribute__((always_inline)) {
      constexpr int q = decltype(Q_)::v, ks = q / 9, qn = q + 8, kn = qn / 9;
      step(Q_, gf[2 + ks / 2][2 * (ks & 1)], gf[2 + ks / 2][2 * (ks & 1) + 1], (kn < 4 ? wo1 + kn * 36 * 288 : wo2 + (kn - 4) * 72 * 288) + (qn % 9) * 32, IC<(qn < 54)>{});
      if constexpr (q == 1 || q == 4 || q == 7 || q == 10) ISSUE_G(4, (q - 1) / 3, 20 * 16384);
      if constexpr (q == 48) { asm volatile("s_nop 3" ::: "memory"); *(LAS f32x4*)(LB + r * 3072 + l * 16) = ac[1][0]; *(LAS f32x4*)(LB + r * 3072 + 1024 + l * 16) = ac[1][1]; }
      if constexpr (q == 50 || q == 52) {
        constexpr int k = (q - 48) / 2;
        asm volatile("s_nop 3" ::: "memory"); fence_sched();
#pragma unroll
        for (int e = 0; e < 2; ++e)
          st16c<0>(u32x4{umin(pk2(ac[e][2 * k][0], ac[e][2 * k][1]), SENT - 1), umin(pk2(ac[e][2 * k][2], ac[e][2 * k][3]), SENT - 1), umin(pk2(ac[e][2 * k + 1][0], ac[e][2 * k + 1][1]), SENT - 1), umin(pk2(ac[e][2 * k + 1][2], ac[e][2 * k + 1][3]), SENT - 1)},
                   sWS, l * 16 + e * 4096, pbase + (bid ^ k) * 27648 + (k - 1) * 9216);
      }
      fence_sched();
    });
    asm volatile("s_nop 7" ::: "memory"); fence_sched();
#pragma unroll
    for (int e = 0; e < 2; ++e)
      st16c<0>(u32x4{umin(pk2(ac[e][6][0], ac[e][6][1]), SENT - 1), umin(pk2(ac[e][6][2], ac[e][6][3]), SENT - 1), umin(pk2(ac[e][7][0], ac[e][7][1]), SENT - 1), umin(pk2(ac[e][7][2], ac[e][7][3]), SENT - 1)},
               sWS, l * 16 + e * 4096, pbase + (bid ^ 3) * 27648 + 2 * 9216);
#pragma unroll
    for (int e = 0; e < 2; ++e)
      st8c<0>(u32x2{umin(pk2(ac[e][8][0], ac[e][8][1]), SENT - 1), umin(pk2(ac[e][8][2], ac[e][8][3]), SENT - 1)}, sWS, l * 8 + e * 512, r == kq ? OOB : WS_P + (bid ^ kq ^ r) * 27648 + ((kq ^ r) - 1) * 9216 + 8192);
    *(LAS f32x4*)(LB + r * 3072 + 2048 + l * 16) = ac[1][8];
    v0 = ac[0][0]; v1 = ac[0][1]; v8 = ac[0][8];
    wait_lgkm0(); bar();
  } else {
#define BARS() bar()
    __builtin_amdgcn_s_setprio(1);
    u32 vb[5];
#pragma unroll
    for (int i = 0; i < 5; ++i) { const int pc = 64 * (r + 4 * i) + l, pp = pc % 18; vb[i] = (pc / 18) * 1152 + (pp < 16 ? pp ^ (4 * kq) : pp) * 16 + 288 * s; }
    const u64 m5 = __builtin_amdgcn_ballot_w64(tid < 384);
    const u32x4 sB = make_srd(arg(5) + 1152 * kq * 1152, 1152 * 1152);
    u32 wso, wd = LB + r * 1024;
    int wn = 0;
    auto issue_w = [&](auto I_) __attribute__((always_inline)) {
      constexpr int i = decltype(I_)::v;
      if constexpr (i == 0) wso = line(wn);
      if constexpr (i < 4) dma16<i * 4096, 0>(sB, vb[i], wso, wd);
      else { dma16m<16384, 0>(sB, vb[4], wso, wd, m5); ++wn; wd = wd >= 4 * SLOT_B ? wd - 4 * SLOT_B : wd + SLOT_B; }
    };
#define ISSUE_W(i) issue_w(IC<(i)>{})
    u32x2 q5[5];
    {
      const u32 qo = WS_QKV + (kq * 16 + m) * QKV_SZ + 9216 * s, qv = (32 * r + (l & 31)) * 8;
      q5[0] = ld8c<0>(sWS, qv + 2 * h * 1024, qo); q5[1] = ld8c<1024>(sWS, qv + 2 * h * 1024, qo);
      q5[2] = ld8c<0>(sWS, qv + 2 * h * 1024, qo + 5120); q5[3] = ld8c<1024>(sWS, qv + 2 * h * 1024, qo + 5120);
      q5[4] = ld8c<0>(sWS, h ? OOB : qv, qo + 4096);
    }
    last_gelu([&](auto G_) __attribute__((always_inline)) {
      ISSUE_W(0); ISSUE_W(1); ISSUE_W(2); ISSUE_W(3); ISSUE_W(4);
    });
    STAMP(11);
#pragma unroll
    for (int i = 0; i < 5; ++i) pin(q5[i]);
    const u32x4 qf[2] = {cat(q5[0], q5[1]), cat(q5[2], q5[3])};
    const u32x2 qq = q5[4];
    const u32 fvo = WS_FLAG + (768 + (bid & ~24)) * 128 + ((l >> 1) & 3) * 1024 + (l & 1) * 4;
    u32 ka = LB + L_KV + (32 * r + (l & 31)) * 8, kb = ka + 4 * PP;
    ka += 2 * h * PP;
    u32 va = LB + L_KV + (32 * r + 4 * h + q4) * 8 + (blk ? (p4 ? 18 : 13) : 9 + p4) * PP;
    u32x4 qm_ = qf[0], qt = {qq[0], 0u, h ? 0u : 0x3f800000u, 0u};
    u32 oso = (36 + s) * 8192, tso = (40 + (s >> 1)) * 8192 + 2 * (s & 1) * 256;
#define K_MAIN(i) as_bf(cat(lds_r8(ka + (i) * 256), lds_r8(ka + PP + (i) * 256)))
#define K_TAIL(i) as_bf(cat(lds_r8(kb + (i) * 256), lds_r8(kb + 14 * PP + (i) * 256)))
#pragma unroll 1
    for (int hd = 0; hd < 2; ++hd) {
      pin(ka); pin(kb); pin(va);
      bf16x8 km[3], kt[3], k8;
      f32x16 st[9];
      km[0] = K_MAIN(0); kt[0] = K_TAIL(0); km[1] = K_MAIN(8); kt[1] = K_TAIL(8); k8 = kt[1];
      float mx0, mx1;
      sfor<0, 11>([&](auto I_) __attribute__((always_inline)) {
        constexpr int ii = decltype(I_)::v;
        if constexpr (ii + 2 < 9) { km[(ii + 2) % 3] = K_MAIN(ii + 1); kt[(ii + 2) % 3] = K_TAIL(ii + 1); }
        if constexpr (ii < 9) {
          st[ii] = MFMA32(km[ii % 3], as_bf(qm_), f32x16{});
          st[ii] = MFMA32(kt[ii % 3], as_bf(qt), st[ii]);
          pin(st[ii]);
        }
        if constexpr (ii < 10) ISSUE_W(ii % 5);
        if constexpr (ii == 3) {
#pragma unroll
          for (int e = 0; e < 16; ++e) st[1][e] = __builtin_amdgcn_inverse_ballot_w64(diag_mask(e)) ? st[1][e] : st[0][e];
          mx0 = st[1][0]; mx1 = st[1][1];
        }
        if constexpr (ii >= 3) {
          constexpr int i = ii - 2;
#pragma unroll
          for (int e = (ii == 3 ? 2 : 0); e < 16; e += 2) { if (e & 2) mx0 = max3(mx0, st[i][e], st[i][e + 1]); else mx1 = max3(mx1, st[i][e], st[i][e + 1]); }
          pin(mx0); pin(mx1);
        }
        fence_sched();
        if constexpr (ii == 4) { wait_vm<8>(); BARS(); if (hd == 0) wait_vm<0>(); }
      });
      float mx;
      { auto sw = __builtin_amdgcn_permlane32_swap(f2u(mx0), f2u(mx1), false, false); mx = max3(u2f(sw[0]), u2f(sw[1]), u2f(sw[0]));
        auto s2 = __builtin_amdgcn_permlane32_swap(f2u(mx), f2u(mx), false, false); mx = max3(u2f(s2[0]), u2f(s2[1]), u2f(s2[0])); }
      u32x4 qm = {};
      { const float n0 = -mx, a0 = bhi(f2u(n0)), n1 = n0 - a0, a1 = bhi(f2u(n1)), n2 = n1 - a1;
        qm[2] = h ? 0u : f2u(a0) >> 16; qm[3] = h ? 0u : ((f2u(a1) >> 16) | (f2u(n2) & 0xffff0000u)); }
      st[1] = MFMA32(k8, as_bf(qm), st[1]); st[2] = MFMA32(k8, as_bf(qm), st[2]); pin(st[1]); pin(st[2]);
      bf16x8 vf[2], v0f[2];
      vf[0] = tr_frag(va + 8 * 256, va + 8 * 256 + 64);
      v0f[0] = tr_frag(va, va + 64); v0f[1] = tr_frag(va + 128, va + 192);
      const u32x4 mk0 = lds_r16(LB + L_MSK + l * 16), mk1 = lds_r16(LB + L_MSK + 1024 + l * 16);
      wait_vm<8>(); BARS();
      if (tid == 256) __hip_atomic_store((GAS u64*)(ws + WS_FLAG + (768 + bid) * 128), tag, RLX_AGENT);
      f32x16 o;
      sfor<0, 16>([&](auto J_) __attribute__((always_inline)) {
        constexpr int j = decltype(J_)::v, i = 1 + (j >> 1), u = j & 1, jn = j + 1, tn = (jn >> 1) == 0 ? 8 : (jn >> 1);
        if constexpr (jn < 16) vf[jn & 1] = tr_frag(va + tn * 256 + (jn & 1) * 128, va + tn * 256 + (jn & 1) * 128 + 64);
        if constexpr (u == 0 && i + 2 < 9) { st[i + 2] = MFMA32(k8, as_bf(qm), st[i + 2]); pin(st[i + 2]); }
        ISSUE_W(j % 4); if constexpr (j % 4 == 3) ISSUE_W(4);
        u32x4 pf;
#pragma unroll
        for (int e = 0; e < 4; ++e) pf[e] = pk2(__builtin_amdgcn_exp2f(st[i][8 * u + 2 * e]), __builtin_amdgcn_exp2f(st[i][8 * u + 2 * e + 1]));
        if constexpr (i == 1) {
          const u32x4 mk = u ? mk1 : mk0;
          if constexpr (j == 0) o = MFMA32(v0f[u], as_bf(pf & ~mk), f32x16{}); else o = MFMA32(v0f[u], as_bf(pf & ~mk), o);
          pf &= mk;
        }
        o = MFMA32(vf[u], as_bf(pf), o); pin(o);
        if constexpr (j % 4 == 1) st16c<0>(u32x4{SENT, SENT, SENT, SENT}, sWS, (j == 13 && hd) || (j == 9 && hd && r == 3) ? OOB : (tid - 256) * 16 + (j >> 2) * 4096, WS_P + bid * 27648 + hd * 16384);
        fence_sched();
        if constexpr (j == 11) {
          wait_vm<5>();
          const u32x2 fl = lds_r8(LB + L_FLG + r * 64 + (l & 3) * 8);
          if (RARE(!all_lanes(fl[0] == (u32)tag && fl[1] == (u32)(tag >> 32)))) poll_words(ws, fvo & ~4u, l < 8, tag);
        }
        if constexpr (j % 4 == 3 && j < 15) { wait_vm<9>(); BARS(); }
        if constexpr (j == 7) fetch_flags(sWS, fvo, LB + L_FLG + r * 64);
      });
      const float inv = __builtin_amdgcn_rcpf(h ? o[8] : o[12]);
      st16c<0>(u32x4{pk2(o[0] * inv, o[1] * inv), pk2(o[2] * inv, o[3] * inv), pk2(o[4] * inv, o[5] * inv), pk2(o[6] * inv, o[7] * inv)}, sG, gvo, oso);
      st16c<0>(u32x4{hd ? pk2(o[10] * inv, o[11] * inv) : pk2(o[8] * inv, o[9] * inv), 0u, 0u, 0u}, sG, h ? OOB : gvo, tso);
      oso += 512; tso += 256;
      touch(make_srd(arg(2) + 144 * kq * 1152, 144 * 1152), l < 6 ? ((((bid >> 3) * 4 + r) * 2 + hd) * 6 + l) * 128 : OOB, LB + L_DUMP);
      ka += 5 * PP; va += blk ? 0 : 5 * PP; qm_ = qf[1]; qt[0] = 0u; qt[1] = qq[1];
      wait_vm<12>(); BARS();
    }
    STAMP(14);
    const u32x4 sWO = make_srd(arg(2) + 144 * kq * 1152, 144 * 1152), sX = make_srd(arg(0), 2048 * 1152);
    const u32 dw = LB + L_KV + r * 1024;
    auto issue_o = [&](auto I_) __attribute__((always_inline)) {
      constexpr int i = decltype(I_)::v, sl = i / 5, k = i % 5;
      if constexpr (k < 4 && i != 11) dma16<sl * SLOT_B + k * 4096, sl * 73728>(sWO, vb[k], 0u, dw); else dma16m<sl * SLOT_B + k * 4096, sl * 73728>(sWO, vb[k], 0u, dw, m5);
    };
    wait_vm<0>();
    sfor<0, 5>([&](auto I_) __attribute__((always_inline)) { ISSUE_W(decltype(I_)::v); });
    BARS();
    if (tid == 256) __hip_atomic_store((GAS u64*)(ws + WS_FLAG + (512 + bid) * 128), tag, RLX_AGENT);
    sfor<0, 5>([&](auto I_) __attribute__((always_inline)) { ISSUE_W(decltype(I_)::v); });
    BARS();
    sfor<0, 7>(issue_o);
    wait_vm<10>(); BARS();
    if (r == 0) poll_words(ws, WS_FLAG + ((l & 4) ? (512 + (bid & ~3) + (l & 3)) * 128 : (512 + (bid & ~24)) * 128 + (l & 3) * 1024), l < 8, tag);
    sfor<7, 12>(issue_o);
    wait_vm<9>(); BARS();
    dma16<L_RES, 0>(sX, (l >> 2) * 1152 + p4 * 16 + 288 * s + 64 * kq, xso - 16 * 1152, LB + 2 * r * 1024);
    dma16<L_RES + 1024, 0>(sX, (l >> 2) * 1152 + p4 * 16 + 288 * s + 64 * kq, xso, LB + 2 * r * 1024);
    if (r == 0) dma16<L_RES + 8192, 0>(sX, (l >> 1) * 1152 + (l & 1) * 16 + 288 * s + 256, (128 * m + 32 * kq) * 1152, LB);
    if (r == 1) dma16<L_RES + 9216, 0>(make_srd(arg(6), 1152), l * 16 + 288 * s, 0, LB);
    wait_vm<2>(); BARS();
    BARS();
    if (bid == 24 && r == 1) {
#pragma unroll 1
      for (int i = 0; i < 4; ++i) poll_words(ws, WS_FLAG + (l + 64 * i) * 128, true, tag);
      if (l == 0) __hip_atomic_store((GAS u64*)(ws + WS_EPOCH), tag, RLX_AGENT);
    }
    wait_vm<0>(); BARS();
    v0 = *(LAS f32x4*)(LB + r * 3072 + l * 16); v1 = *(LAS f32x4*)(LB + r * 3072 + 1024 + l * 16); v8 = *(LAS f32x4*)(LB + r * 3072 + 2048 + l * 16);
  }
  STAMP(16);
  {
    const bool own8 = r == kq;
    const u32 ra = LB + L_RES + (2 * r + c) * 1024 + t16 * 64 + g4 * 8, r8 = LB + L_RES + 8192 + (16 * c + t16) * 32 + g4 * 8, rb = LB + L_RES + 9216 + g4 * 8;
    {
      const u32x2 x0 = lds_r8(ra), x1 = lds_r8(ra + 32), x8 = lds_r8(r8), b0 = lds_r8(rb + 64 * kq), b1_ = lds_r8(rb + 64 * kq + 32), b8 = lds_r8(rb + 256);
      v0 += f32x4{blo(x0[0]), bhi(x0[0]), blo(x0[1]), bhi(x0[1])} + f32x4{blo(b0[0]), bhi(b0[0]), blo(b0[1]), bhi(b0[1])};
      v1 += f32x4{blo(x1[0]), bhi(x1[0]), blo(x1[1]), bhi(x1[1])} + f32x4{blo(b1_[0]), bhi(b1_[0]), blo(b1_[1]), bhi(b1_[1])};
      v8 += f32x4{blo(x8[0]), bhi(x8[0]), blo(x8[1]), bhi(x8[1])} + f32x4{blo(b8[0]), bhi(b8[0]), blo(b8[1]), bhi(b8[1])};
      pin(v0); pin(v1); pin(v8);
    }
    const u32x4 sOUT = make_srd(arg(7), 2048 * 1152);
    u32 ov = (l >> 2) * 1152 + p4 * 16 + 288 * s + 64 * kq, ov8 = (own8 && l < 32) ? (l >> 1) * 1152 + (l & 1) * 16 + 288 * s + 256 : OOB;
    pin(ov); pin(ov8);
    const u32 po = WS_P + bid * 27648;
    u32x4 pa[3]; u32x2 pb[3];
    STAMP(19);
#pragma unroll 1
    for (u32 spins = 0; spins < SPINS; ++spins) {
      asm volatile("" ::: "memory");
#pragma unroll
      for (int sg = 0; sg < 3; ++sg) {
        pa[sg] = __builtin_amdgcn_raw_buffer_load_b128(rWS, l * 16, po + sg * 9216 + w * 1024, 16);
        pb[sg] = __builtin_amdgcn_raw_buffer_load_b64(rWS, l * 8, own8 ? po + sg * 9216 + 8192 + c * 512 : OOB, 16);
      }
      u32 mxu = 0;
#pragma unroll
      for (int sg = 0; sg < 3; ++sg) mxu = umax(umax(umax(mxu, umax(pa[sg][0], pa[sg][1])), umax(pa[sg][2], pa[sg][3])), umax(pb[sg][0], pb[sg][1]));
      if (all_lanes(mxu != SENT)) break;
      __builtin_amdgcn_s_sleep(1);
    }
    STAMP(20);
#pragma unroll
    for (int sg = 0; sg < 3; ++sg) {
      v0 += f32x4{blo(pa[sg][0]), bhi(pa[sg][0]), blo(pa[sg][1]), bhi(pa[sg][1])};
      v1 += f32x4{blo(pa[sg][2]), bhi(pa[sg][2]), blo(pa[sg][3]), bhi(pa[sg][3])};
    }
    *(LAS u32x2*)ra = u32x2{pk2(v0[0], v0[1]), pk2(v0[2], v0[3])};
    *(LAS u32x2*)(ra + 32) = u32x2{pk2(v1[0], v1[1]), pk2(v1[2], v1[3])};
    const u32x4 t = lds_r16(LB + L_RES + (2 * r + c) * 1024 + l * 16);
    asm volatile("s_nop 4\n\tbuffer_store_dwordx4 %0, %1, %2, %3 offen\n\ts_nop 1" :: "v"(t), "v"(ov), "s"(sOUT), "s"(xso) : "memory");
    if (own8) {
#pragma unroll
      for (int sg = 0; sg < 3; ++sg) v8 += f32x4{blo(pb[sg][0]), bhi(pb[sg][0]), blo(pb[sg][1]), bhi(pb[sg][1])};
      *(LAS u32x2*)r8 = u32x2{pk2(v8[0], v8[1]), pk2(v8[2], v8[3])};
      const u32x4 t8 = lds_r16(LB + L_RES + 8192 + c * 512 + (l & 31) * 16);
      asm volatile("s_nop 4\n\tbuffer_store_dwordx4 %0, %1, %2, %3 offen\n\ts_nop 1" :: "v"(t8), "v"(ov8), "s"(sOUT), "s"(xso) : "memory");
    }
  }
}
#if defined(__HIP_DEVICE_COMPILE__)
#pragma clang attribute pop
#endif

extern "C" void kernel_launch(void* const* d_in, const int* in_sizes, int n_in, void* d_out, int out_size, void* d_ws, size_t ws_size, hipStream_t stream) {
  fused_block<<<256, 512, 0, stream>>>((const u32*)d_in[0], (const u32*)d_in[1], (const u32*)d_in[2], (const u32*)d_in[4], (const u32*)d_in[5],
                                       (const u32*)d_in[6], (const u32*)d_in[7], (u32*)d_out, (char*)d_ws);
}
```

```cpp
#include <hip/hip_runtime.h>
#pragma clang diagnostic ignored "-Wint-to-pointer-cast"
#if defined(__HIP_DEVICE_COMPILE__)
#pragma clang attribute push (__attribute__((target("no-packed-fp32-ops"))), apply_to = function)
#endif
typedef unsigned u32;
typedef unsigned long long u64;
typedef short s16x4 __attribute__((ext_vector_type(4)));
typedef __bf16 bf16x8 __attribute__((ext_vector_type(8)));
typedef __bf16 bf16x2 __attribute__((ext_vector_type(2)));
typedef float f32x2 __attribute__((ext_vector_type(2)));
typedef float f32x4 __attribute__((ext_vector_type(4)));
typedef float f32x16 __attribute__((ext_vector_type(16)));
typedef u32 u32x2 __attribute__((ext_vector_type(2)));
typedef u32 u32x4 __attribute__((ext_vector_type(4)));
#define LAS __attribute__((address_space(3)))
#define GAS __attribute__((address_space(1)))
#define DEV __device__ __forceinline__
#define RLX_AGENT __ATOMIC_RELAXED, __HIP_MEMORY_SCOPE_AGENT

constexpr u32 WS_EPOCH = 0;
constexpr u32 WS_FLAG = 1024;
constexpr u32 QKV_SZ = 3 * 36 * 1024;
constexpr u32 WS_QKV = 262144;
constexpr u32 G_SZ = 41 * 8192;
constexpr u32 WS_G = WS_QKV + 64 * QKV_SZ;
constexpr u32 WS_P = WS_G + 64 * G_SZ;
constexpr u32 WS_END = WS_P + 256 * 27648;
constexpr u32 OOB = 0x7ffffff0u;
constexpr u32 PP = 3104;
constexpr u32 L_KV = 98304, L_BIAS = 157312, L_RS = 159360, L_MSK = 159872, L_DUMP = 161920, L_FLG = 162176, L_TAB = 162688, L_TOTAL = 163840;
constexpr u32 SLOT_A = 24576, SLOT_B = 18432, L_RES = L_KV + 43008;
constexpr u32 SENT = 0xffffffffu;
constexpr u32 CODE_PF = 29440;

DEV u32x4 make_srd(const void* p, u32 bytes) { u64 a = (u64)p; return u32x4{(u32)a, (u32)(a >> 32) & 0xffffu, bytes, 0x00020000u}; }
template <int LI, int SI> DEV void dma16(u32x4 srd, u32 voff, u32 soff, u32 lds) {
  u32 t;
  asm volatile("s_add_u32 m0, %1, %5\n\ts_add_u32 %0, %4, %6\n\tbuffer_load_dwordx4 %2, %3, %0 offen lds"
               : "=&s"(t) : "s"(lds), "v"(voff), "s"(srd), "s"(soff), "i"(LI), "i"(SI) : "memory", "scc");
}
template <int LI> DEV void dma16s(u32x4 srd, u32 voff, u32& so, u32 inc, u32 lds) {
  asm volatile("s_add_u32 m0, %1, %5\n\ts_add_u32 %0, %0, %4\n\tbuffer_load_dwordx4 %2, %3, %0 offen sc1 lds"
               : "+s"(so) : "s"(lds), "v"(voff), "s"(srd), "s"(inc), "i"(LI) : "memory", "scc");
}
DEV void touch(u32x4 srd, u32 voff, u32 lds) {
  asm volatile("s_mov_b32 m0, %0\n\ts_nop 0\n\tbuffer_load_dword %1, %2, 0 offen sc1 lds" :: "s"(lds), "v"(voff), "s"(srd) : "memory");
}
DEV void fetch_flags(u32x4 srd, u32 voff, u32 lds) {
  asm volatile("s_mov_b32 m0, %0\n\ts_mov_b64 exec, 0xffff\n\tbuffer_load_dword %1, %2, 0 offen sc1 lds\n\ts_mov_b64 exec, -1" :: "s"(lds), "v"(voff), "s"(srd) : "memory");
}
template <int LI, int SI> DEV void dma16m(u32x4 srd, u32 voff, u32 soff, u32 lds, u64 mask) {
  u32 t;
  asm volatile("s_add_u32 m0, %1, %6\n\ts_add_u32 %0, %4, %7\n\ts_mov_b64 exec, %5\n\tbuffer_load_dwordx4 %2, %3, %0 offen lds\n\ts_mov_b64 exec, -1"
               : "=&s"(t) : "s"(lds), "v"(voff), "s"(srd), "s"(soff), "s"(mask), "i"(LI), "i"(SI) : "memory", "scc");
}
template <int OFF> DEV u32x4 ld16(u32x4 srd, u32 voff, u32 soff) {
  u32x4 r; asm volatile("buffer_load_dwordx4 %0, %1, %2, %3 offen offset:%4" : "=v"(r) : "v"(voff), "s"(srd), "s"(soff), "i"(OFF) : "memory"); return r;
}
template <int OFF> DEV u32x2 ld8c(u32x4 srd, u32 voff, u32 soff) {
  u32x2 r; asm volatile("buffer_load_dwordx2 %0, %1, %2, %3 offen offset:%4 sc1" : "=v"(r) : "v"(voff), "s"(srd), "s"(soff), "i"(OFF) : "memory"); return r;
}
template <int OFF> DEV void st16c(u32x4 v, u32x4 srd, u32 voff, u32 soff) {
  asm volatile("buffer_store_dwordx4 %0, %1, %2, %3 offen offset:%4 sc1\n\ts_nop 1" :: "v"(v), "v"(voff), "s"(srd), "s"(soff), "i"(OFF) : "memory");
}
template <int OFF> DEV void st8c(u32x2 v, u32x4 srd, u32 voff, u32 soff) {
  asm volatile("buffer_store_dwordx2 %0, %1, %2, %3 offen offset:%4 sc1\n\ts_nop 1" :: "v"(v), "v"(voff), "s"(srd), "s"(soff), "i"(OFF) : "memory");
}
template <class T> DEV void pins(T& v) { asm volatile("" : "+s"(v)); }
DEV const char* arg(int i) {
  const u64 __attribute__((address_space(4)))* ka = (const u64 __attribute__((address_space(4)))*)__builtin_amdgcn_kernarg_segment_ptr();
  asm volatile("" : "+s"(ka));
  return (const char*)ka[i];
}
template <class T> DEV void pin(T& v) { asm volatile("" : "+v"(v)); }
template <int N> DEV void wait_vm() { asm volatile("s_waitcnt vmcnt(%0)" :: "i"(N) : "memory"); }
DEV void wait_lgkm0() { asm volatile("s_waitcnt lgkmcnt(0)" ::: "memory"); }
DEV void fence_sched() { __builtin_amdgcn_sched_barrier(0); }
DEV void bar() { fence_sched(); __builtin_amdgcn_s_barrier(); fence_sched(); }

DEV float u2f(u32 v) { return __builtin_bit_cast(float, v); }
DEV u32 f2u(float v) { return __builtin_bit_cast(u32, v); }
DEV bf16x8 as_bf(u32x4 v) { return __builtin_bit_cast(bf16x8, v); }
DEV u32 pk2(float a, float b) { return __builtin_bit_cast(u32, __builtin_convertvector(f32x2{a, b}, bf16x2)); }
DEV float blo(u32 d) { return u2f(d << 16); }
DEV float bhi(u32 d) { return u2f(d & 0xffff0000u); }
DEV float rbf(float f) { return blo(pk2(f, f)); }
DEV float max3(float a, float b, float c) { return __builtin_elementwise_maximum(__builtin_elementwise_maximum(a, b), c); }
DEV u32 umax(u32 a, u32 b) { return a > b ? a : b; }
DEV u32 umin(u32 a, u32 b) { return a < b ? a : b; }
DEV bool all_lanes(bool p) { return __builtin_amdgcn_ballot_w64(!p) == 0; }
DEV float dot2(u32 a, u32 b, float c) { return __builtin_amdgcn_fdot2_f32_bf16(__builtin_bit_cast(bf16x2, a), __builtin_bit_cast(bf16x2, b), c, false); }
DEV float quad_sum(float v) {
  v += u2f(__builtin_amdgcn_mov_dpp(f2u(v), 0xb1, 0xf, 0xf, true));
  return v + u2f(__builtin_amdgcn_mov_dpp(f2u(v), 0x4e, 0xf, 0xf, true));
}
DEV u32x2 lds_r8(u32 a) { return *(volatile LAS u32x2*)a; }
DEV u32x4 cat(u32x2 a, u32x2 b) { return u32x4{a[0], a[1], b[0], b[1]}; }
DEV u32x4 lds_r16(u32 a) { return *(LAS u32x4*)a; }
DEV void lds_w16(u32 a, u32x4 v) { *(LAS u32x4*)a = v; }
DEV bf16x8 tr_frag(u32 a0, u32 a1) {
  s16x4 lo = __builtin_amdgcn_ds_read_tr16_b64_v4i16((LAS s16x4*)a0);
  s16x4 hi = __builtin_amdgcn_ds_read_tr16_b64_v4i16((LAS s16x4*)a1);
  return __builtin_bit_cast(bf16x8, __builtin_shufflevector(lo, hi, 0, 1, 2, 3, 4, 5, 6, 7));
}
template <int I> struct IC { static constexpr int v = I; };
template <int I, int N, class F> DEV void sfor(F&& f) { if constexpr (I < N) { f(IC<I>{}); sfor<I + 1, N>(f); } }
#define SPINS (1u << 18)
#define RARE(x) __builtin_expect(!!(x), 0)
#define MFMA32(a, b, c) __builtin_amdgcn_mfma_f32_32x32x16_bf16((a), (b), (c), 0, 0, 0)
DEV void mfma16_acc(f32x4& c, bf16x8 a, u32x4 b) { asm volatile("v_mfma_f32_16x16x32_bf16 %0, %1, %2, %0" : "+v"(c) : "v"(a), "v"(b)); }
#define MFMA16(a, b, c) __builtin_amdgcn_mfma_f32_16x16x32_bf16((a), (b), (c), 0, 0, 0)

DEV float gelu_a(float x) { return __builtin_amdgcn_exp2f(x * __builtin_fmaf(x * x, -0.10294324f, -2.3022082f)); }
DEV float gelu_b(float x, float e) { return x * __builtin_amdgcn_rcpf(1.f + e); }
DEV void poll_words(const char* ws, u32 off, bool active, u64 tag) {
#pragma unroll 1
  for (u32 spins = 0; spins < SPINS; ++spins) {
    u64 v = active ? __hip_atomic_load((GAS u64*)(ws + off), RLX_AGENT) : tag;
    if (all_lanes(v == tag)) break;
    __builtin_amdgcn_s_sleep(2);
  }
  __builtin_amdgcn_fence(__ATOMIC_ACQUIRE, "wavefront");
}
DEV u32 mul24(u32 a, u32 b) { u32 d; asm("v_mul_u32_u24 %0, %1, %2" : "=v"(d) : "s"(b), "v"(a)); return d; }
DEV u32 w2_piece(u32 pc, int kq, int s) {
  const u32 row = mul24(pc, 3641) >> 16, pp = pc - mul24(row, 18);
  return mul24(row, 1152) + (pp < 16 ? pp ^ (4 * kq) : pp) * 16 + 288 * s;
}
DEV int bid_of(int m, int kq, int s) { return ((m & 7) * 4 + s) * 8 + (m >> 3) * 4 + kq; }
constexpr int line_of(int s, int n) {
  const int LG[4] = {0, 4, 8, 14};
  return n < 4 ? LG[(s + n) & 3] : n < 6 ? n + 8 : n < 18 ? LG[(s + n / 3 - 2) & 3] + 1 + n % 3 : n;
}
constexpr u64 line_tab(int s, int half) { u64 t = 0; for (int i = 0; i < 12; ++i) t |= (u64)line_of(s, 12 * half + i) << (5 * i); return t; }
constexpr int o_ks(int q) { return q < 9 ? 0 : q < 37 ? 1 + (q - 9) / 7 : 1 + (q - 37) / 2; }
constexpr int o_d(int q) { return q < 9 ? q : q < 37 ? 2 + (q - 9) % 7 : (q - 37) % 2; }
constexpr u64 diag_mask(int e) {
  u64 mk = 0;
  for (int l = 0; l < 64; ++l) if ((e & 3) + 8 * (e >> 2) + 4 * (l >> 5) <= (l & 31)) mk |= 1ull << l;
  return mk;
}

__global__ __launch_bounds__(512, 2) void fused_block(const u32* __restrict__ x, const u32* __restrict__ wqkv, const u32* __restrict__ wo,
                                                      const u32* __restrict__ w1, const u32* __restrict__ b1, const u32* __restrict__ w2,
                                                      const u32* __restrict__ b2, u32* __restrict__ out, char* __restrict__ ws) {
  __shared__ __attribute__((aligned(1024))) char smem[L_TOTAL];
  const u32 LB = (u32)(size_t)(LAS char*)smem;
  const int tid = __builtin_amdgcn_workitem_id_x(), l = tid & 63;
  const int w = __builtin_amdgcn_readfirstlane(tid >> 6) & 7, r = w & 3, c = w >> 2;
  const int bid = __builtin_amdgcn_workgroup_id_x(), kq = bid & 3, mh = (bid >> 2) & 1, m = 8 * mh + (bid >> 5), s = (bid >> 3) & 3;
  const int h = l >> 5, t16 = l & 15, g4 = l >> 4, q4 = (l & 15) >> 2, p4 = l & 3, blk = (l >> 4) & 1;
  {
    u64 pc; asm volatile("s_getpc_b64 %0" : "=s"(pc));
    touch(make_srd((const void*)(pc & ~127ull), CODE_PF), l < 1 ? ((bid >> 3) + 32 * w) * 128 : OOB, LB + L_DUMP);
  }
  const u32 xso = (128 * m + 32 * r + 16 * c) * 1152;
  const u32x4 sWS = make_srd(ws, WS_END);
  u32x4 xr[18];
  u32x2 ep; u32 braw;
  const int cx = c ^ (s & 1);
  const u32x4 sX0 = make_srd(x, 2048 * 1152);
  const u32 xvo = (l >> 2) * 1152 + p4 * 16, xlo = (128 * m + 32 * r + 16 * cx) * 1152;
#define XL(i) xr[i] = ld16<64 * (i)>(sX0, xvo, xlo);
  XL(0) XL(1) XL(2) XL(3) XL(4) XL(5) XL(6) XL(7) XL(8) XL(9)
  const bool isq = s < 3;
  const int NC = isq ? 7 : 6, NQ = isq ? 3 : 0;
  const int ql0 = 2 * kq + (kq < 2 ? 2 : 0), ql1 = 2 * kq + (kq < 2 ? 0 : 1);
  const u32x4 sW1 = make_srd(w1, 576 * 9216);
  const u32 pcs = 16 * ((l & 7) ^ (4 * blk));
  const u32 vo_f = (l >> 3) * 9216 + pcs;
  const u32 dl = LB + w * 3072;
  const u32 fcol = 2304 * kq + 512 * s;
  u32x4 sA = isq ? make_srd(wqkv, 576 * 3456) : sW1;
  u32 voA = isq ? ((l >> 3) * 3456 + pcs) ^ (kq < 2 ? 0 : 64) : vo_f, i8 = isq ? 8 * 3456 : 8 * 9216, i176 = 22 * i8;
  u32 so = 3 * w * i8 + (isq ? 1152 * s + 128 * ql0 : fcol) - i176;
#define DMA_A(i, lds) dma16s<(i) * 1024>(sA, voA, so, (i) == 0 ? i176 : i8, lds)
  if (w == 1 && l < 32) {
    const u64 T0 = s == 0 ? line_tab(0, 0) : s == 1 ? line_tab(1, 0) : s == 2 ? line_tab(2, 0) : line_tab(3, 0);
    const u64 T1 = s == 0 ? line_tab(0, 1) : s == 1 ? line_tab(1, 1) : s == 2 ? line_tab(2, 1) : line_tab(3, 1);
    *(LAS u32*)(LB + L_TAB + l * 4) = (u32)(l < 12 ? T0 >> (5 * l) : T1 >> ((5 * l - 60) & 63)) & 31;
  }
  if (w == 0) {
    u32x4 mk[2];
#pragma unroll
    for (int e = 0; e < 8; ++e) { const int d = (l & 31) - 4 * h - ((2 * e & 3) + 8 * (e >> 1)); mk[e >> 2][e & 3] = d < 0 ? 0u : (d == 0 ? 0xffffu : ~0u); }
    lds_w16(LB + L_MSK + l * 16, mk[0]); lds_w16(LB + L_MSK + 1024 + l * 16, mk[1]);
  }
  u32 tv0, tv1;
  {
    const int wi = (m & 7) * 8 + w, ln = mul24(l, 57) >> 9, trow = wi * 9 + l - 9 * ln;
    tv0 = ln < 2 ? mul24(trow, i8 >> 3) + (isq ? 1152 * s + 128 * (ql1 + ln) : fcol + 128 * (1 + ln)) : (l < 21 ? mul24(384 + wi * 3 + l - 18, i8 >> 3) + (isq ? 1152 * s + 128 * ql0 : fcol) : OOB);
    tv1 = ln < (isq ? 4 : 3) ? mul24(trow, 9216) + fcol + 128 * (ln + (isq ? 0 : 3)) : OOB;
    pin(tv0); pin(tv1);
  }
  u64 tag;
  u32x4 xf[36];
  float rs;
  const u32x4 sG = make_srd(ws + WS_G + (kq * 16 + m) * G_SZ, G_SZ);
  f32x16 prev;
  u32 ba = LB + L_BIAS + c * 128 + h * 16;
  u32 hso = (8 * s + c) * 8192, gvo;
  {
    const u32 wl = LB + h * 1024 + q4 * 128 + 64 * (c ^ ((q4 >> 1) & 1)) + 32 * blk + 8 * p4;
    constexpr int PF = 4;
    bf16x8 wf[PF];
    f32x16 acc;
    {
      u32 xwa = LB + p4 * 512 + (((16 * cx + (l >> 2)) ^ (p4 << 1)) << 4), xra = LB + h * 512 + (((l & 31) ^ (h << 1)) << 4);
      u32 xwb = xwa + L_KV + r * 12288, xrc = xra + L_KV + r * 12288, xrd = xrc ^ 64;
      xwa += 2 * SLOT_A + r * 24576; xra += 2 * SLOT_A + r * 24576;
      u32 xrb = xra ^ 64;
      pin(xwa); pin(xra); pin(xrb);
      {
        u32 bo = tid < 192 ? fcol + 4 * tid : OOB;
        u32 to1 = w == 7 ? (l < 2 ? mul24((bid >> 3) * 2 + l, 36864) : OOB) : (l < 3 ? (1 + ((bid >> 3) * 7 + w) * 3 + l) << 16 : OOB);
        u32x4 sT = w == 7 ? make_srd(out, 2048 * 1152) : sWS, sB1 = make_srd(b1, 9216);
        u32 wl_ = wl;
        pin(bo); pin(to1); pins(sT); pins(sB1); pin(voA); pins(so); pins(sA); pin(xwb); pin(xrc); pin(xrd); pin(wl_);
        XL(10) XL(11) XL(12) XL(13) XL(14) XL(15) XL(16) XL(17)
#undef XL
        asm volatile("buffer_load_dwordx2 %0, %1, %2, 0 offen sc1" : "=v"(ep) : "v"(WS_EPOCH), "s"(sWS) : "memory");
        asm volatile("buffer_load_dword %0, %1, %2, 0 offen" : "=v"(braw) : "v"(bo), "s"(sB1) : "memory");
        touch(sT, to1, LB + L_DUMP);
        if (c) __builtin_amdgcn_s_setprio(1);
      }
      float s0 = 0.f, s1 = 0.f, q0 = 0.f, q1 = 0.f;
      auto sq = [&](int i) { q0 = dot2(xr[i][0], xr[i][0], q0); q1 = dot2(xr[i][1], xr[i][1], q1); q0 = dot2(xr[i][2], xr[i][2], q0); q1 = dot2(xr[i][3], xr[i][3], q1); };
#pragma unroll
      for (int T = 0; T < 6; ++T) {
        if (T == 0) wait_vm<18>(); else if (T == 1) wait_vm<15>(); else if (T == 2) wait_vm<12>(); else if (T == 3) wait_vm<9>(); else if (T == 4) wait_vm<6>(); else wait_vm<3>();
#pragma unroll
        for (int i = 3 * T; i < 3 * T + 3; ++i) {
          pin(xr[i]);
          s0 = dot2(xr[i][0], 0x3f803f80u, s0); s1 = dot2(xr[i][1], 0x3f803f80u, s1); s0 = dot2(xr[i][2], 0x3f803f80u, s0); s1 = dot2(xr[i][3], 0x3f803f80u, s1);
        }
        fence_sched();
      }
      const float mean = quad_sum(s0 + s1) * (1.f / 576.f), nmean = -rbf(mean);
      auto centre = [&](int i) {
        float d0, d1, d2, d3, d4, d5, d6, d7; u32 o0, o1, o2, o3;
        asm volatile("s_nop 1\n\tv_dot2_f32_bf16 %4, %12, %17, %16\n\tv_dot2_f32_bf16 %5, %12, %18, %16\n\tv_dot2_f32_bf16 %6, %13, %17, %16\n\tv_dot2_f32_bf16 %7, %13, %18, %16\n\t"
                     "v_dot2_f32_bf16 %8, %14, %17, %16\n\tv_dot2_f32_bf16 %9, %14, %18, %16\n\tv_dot2_f32_bf16 %10, %15, %17, %16\n\tv_dot2_f32_bf16 %11, %15, %18, %16\n\t"
                     "v_cvt_pk_bf16_f32 %0, %4, %5\n\tv_cvt_pk_bf16_f32 %1, %6, %7\n\tv_cvt_pk_bf16_f32 %2, %8, %9\n\tv_cvt_pk_bf16_f32 %3, %10, %11"
                     : "=v"(o0), "=v"(o1), "=v"(o2), "=v"(o3), "=&v"(d0), "=&v"(d1), "=&v"(d2), "=&v"(d3), "=&v"(d4), "=&v"(d5), "=&v"(d6), "=&v"(d7)
                     : "v"(xr[i][0]), "v"(xr[i][1]), "v"(xr[i][2]), "v"(xr[i][3]), "v"(nmean), "s"(0x00003f80u), "s"(0x3f800000u));
        xr[i] = u32x4{o0, o1, o2, o3};
      };
#pragma unroll
      for (int i = 0; i < 12; ++i) {
        if (i < 6) { if (i % 3 == 0) DMA_A(0, dl + (i / 3) * SLOT_A); else if (i % 3 == 1) DMA_A(1, dl + (i / 3) * SLOT_A); else DMA_A(2, dl + (i / 3) * SLOT_A); }
        if (i == 6) touch(sA, tv0, LB + L_DUMP);
        centre(i); lds_w16(xwa + i * 2048, xr[i]);
      }
      auto stats = [&]() __attribute__((always_inline)) {
#pragma unroll
        for (int i = 0; i < 18; ++i) sq(i);
        const float dm = mean + nmean;
        const float var = __builtin_fmaxf(quad_sum(q0 + q1) * (1.f / 576.f) - dm * dm, 0.f);
        const float rsd = __builtin_amdgcn_rcpf(rbf(__builtin_amdgcn_sqrtf(rbf(rbf(var) + 1e-5f))));
        if (p4 == 0) *(LAS float*)(LB + L_RS + (32 * r + 16 * cx + (l >> 2)) * 4) = rsd;
      };
      if (c == 0) {
#pragma unroll
        for (int i = 12; i < 18; ++i) centre(i);
        stats();
      }
      wait_lgkm0(); bar();
#pragma unroll
      for (int j = 0; j < 24; ++j) xf[j] = lds_r16((j & 1 ? xrb : xra) + j * 1024);
      fence_sched();
      if (c) {
#pragma unroll
        for (int i = 12; i < 18; ++i) centre(i);
        stats();
      }
      wait_vm<1>();
      pin(ep); pin(braw);
      if (tid < 256) *(LAS f32x2*)(LB + L_BIAS + tid * 8) = f32x2{blo(braw), bhi(braw)};
      tag = (((u64)(u32)__builtin_amdgcn_readfirstlane(ep[1]) << 32) | (u32)__builtin_amdgcn_readfirstlane(ep[0])) + 1;
      pin(xwb); pin(xrc); pin(xrd);
      __builtin_amdgcn_s_setprio(0);
      wait_lgkm0(); bar();
      const bool act = !(isq && c);
#pragma unroll
      for (int j = 0; j < PF; ++j) wf[j] = tr_frag(wl + j * 2048, wl + j * 2048 + 512);
#pragma unroll
      for (int i = 0; i < 6; ++i) lds_w16(xwb + i * 2048, xr[12 + i]);
      DMA_A(0, dl + 2 * SLOT_A); DMA_A(1, dl + 2 * SLOT_A); DMA_A(2, dl + 2 * SLOT_A);
      if (isq) { voA = (l >> 3) * 3456 + pcs; so += 128 * (ql1 - ql0) - 576 * 3456; } else so += 128 - 576 * 9216;
      sfor<0, 9>([&](auto B_) __attribute__((always_inline)) {
        constexpr int kb4 = decltype(B_)::v;
        if (act) {
          sfor<4 * kb4, 4 * kb4 + 4>([&](auto KK) __attribute__((always_inline)) {
            constexpr int kk = decltype(KK)::v;
            if constexpr (kk == 0) acc = MFMA32(wf[0], as_bf(xf[0]), f32x16{});
            else if constexpr (kk < 35) acc = MFMA32(wf[kk % PF], as_bf(xf[kk]), acc);
            else prev = MFMA32(wf[kk % PF], as_bf(xf[kk]), acc);
            if constexpr (kk < 35) pin(acc); else pin(prev);
            fence_sched();
            if constexpr (kk < 32) { const u32 pa = wl + ((kk + PF) / 12) * SLOT_A + ((kk + PF) % 12) * 2048; wf[kk % PF] = tr_frag(pa, pa + 512); }
            fence_sched();
          });
        }
        if constexpr (kb4 % 3 == 0) { DMA_A(0, dl + ((kb4 / 3 + 3) & 3) * SLOT_A); DMA_A(1, dl + ((kb4 / 3 + 3) & 3) * SLOT_A); DMA_A(2, dl + ((kb4 / 3 + 3) & 3) * SLOT_A); }
        if constexpr (kb4 == 3 || kb4 == 4) {
#pragma unroll
          for (int j = 6 * (kb4 - 3); j < 6 * (kb4 - 3) + 6; ++j) xf[24 + j] = lds_r16((j & 1 ? xrd : xrc) + j * 1024);
        }
        if constexpr (kb4 == 2) { asm volatile("s_waitcnt lgkmcnt(8)" ::: "memory"); if (!act) wait_lgkm0(); bar(); }
        if constexpr (kb4 == 4) { wait_vm<6>(); bar(); }
        if constexpr (kb4 == 5) { bar(); }
        if constexpr (kb4 == 8) {
          touch(sW1, tv1, LB + L_DUMP);
          so += 128 - 72 * i8;
          wait_vm<4>(); wait_lgkm0(); bar();
#pragma unroll
          for (int j = 0; j < PF; ++j) wf[j] = tr_frag(wl + 3 * SLOT_A + j * 2048, wl + 3 * SLOT_A + j * 2048 + 512);
          rs = *(LAS float*)(LB + L_RS + (32 * r + (l & 31)) * 4);
        }
        fence_sched();
      });
    }
    u32 R0 = 3 * SLOT_A, R1 = 0, R2 = SLOT_A, R3 = 2 * SLOT_A;
    u32 D0 = dl + R3, D1 = dl + R0, D2 = dl + R1;
    auto kstep = [&](auto KK) __attribute__((always_inline)) {
      constexpr int kk = decltype(KK)::v, sl = kk / 12, ks = kk % 12;
      if constexpr (kk == 0) acc = MFMA32(wf[0], as_bf(xf[0]), f32x16{});
      else if constexpr (kk < 35) acc = MFMA32(wf[ks % PF], as_bf(xf[kk]), acc);
      else asm volatile("v_mfma_f32_32x32x16_bf16 %0, %1, %2, %3" : "=&v"(prev) : "v"(wf[ks % PF]), "v"(xf[kk]), "v"(acc));
      if constexpr (kk < 35) pin(acc);
      fence_sched();
      constexpr int sn = (kk + PF) / 12;
      const u32 pa = wl + (sn == 0 ? R0 : sn == 1 ? R1 : sn == 2 ? R2 : R3) + ((ks + PF) % 12) * 2048;
      wf[ks % PF] = tr_frag(pa, pa + 512);
      fence_sched();
      const u32 dst = sl == 0 ? D0 : sl == 1 ? D1 : D2;
      if constexpr (ks == 1) DMA_A(0, dst);
      if constexpr (ks == 5) DMA_A(1, dst);
      if constexpr (ks == 9) DMA_A(2, dst);
      if constexpr (kk == 34) { D2 = D1; D1 = D0; D0 = dl + R2; }
      if constexpr (kk == 35) { const u32 t = R3; R3 = R2; R2 = R1; R1 = R0; R0 = t; }
    };
    if (isq) {
      const u32 qvo = h * 1024 + (32 * r + (l & 31)) * 8;
      const u32x4 sQ = make_srd(ws + WS_QKV + (kq * 16 + m) * QKV_SZ + s * 36864, 36864);
      int oq = c ? -65536 : (64 * ql0 + (kq < 2 ? 0 : 32) - 144 * kq) * 256, oqn = (64 * ql1 + 32 * c - 144 * kq) * 256;
      const u32x4 sW2 = make_srd(arg(5) + 1152 * kq * 1152, 1152 * 1152);
      u32 tv2 = l < 18 ? ((((m & 7) * 3 + s) * 8 + w) * 54 + l) * 128 : OOB;
      const float re = s == 0 ? rs * 0.34004557f : rs;
#pragma unroll 1
      for (int ci = 1; ci <= 3; ++ci) {
        u32x2 un;
        sfor<0, 36>([&](auto KK) __attribute__((always_inline)) {
          constexpr int kk = decltype(KK)::v;
          if constexpr (kk == 33) { if (RARE(ci == 3)) wait_vm<0>(); }
          kstep(KK);
          if constexpr (kk == 2 || kk == 3 || kk == 6 || kk == 7 || kk == 14 || kk == 15 || kk == 18 || kk == 19) {
            constexpr int e = (kk & 1) + (kk >= 6 ? 2 : 0) + (kk >= 14 ? 2 : 0) + (kk >= 18 ? 2 : 0);
            u32 v = pk2(prev[2 * e] * re, prev[2 * e + 1] * re); pin(v); un[e & 1] = v;
          }
          if constexpr (kk == 4 || kk == 10 || kk == 16 || kk == 22) {
            const int o = kk < 12 ? oq : oq + 4096;
            if constexpr (kk == 4 || kk == 16) st8c<0>(un, sQ, qvo, o < 0 ? OOB : (u32)o); else st8c<2048>(un, sQ, qvo, o < 0 ? OOB : (u32)o);
          }
          if constexpr (kk == 34) {
            touch(sW2, tv2, LB + L_DUMP); tv2 += 18 * 128;
            oq = oqn; oqn += 16384;
            so += 128 - 72 * i8;
            if (RARE(ci == 1)) { sA = sW1; voA = vo_f; i8 = 8 * 9216; i176 = 176 * 9216; so = 24 * w * 9216 + fcol - 176 * 9216; }
          }
          fence_sched();
          if constexpr (kk % 12 == 11) { if constexpr (kk == 11) wait_vm<6>(); else if constexpr (kk == 23) wait_vm<6>(); else wait_vm<5>(); bar(); }
        });
      }
    }
    if (tid == 0) __hip_atomic_store((GAS u64*)(ws + WS_FLAG + bid * 128), tag, RLX_AGENT);
    gvo = 2 * r * 1024 + blk * 1024 + h * 256 + t16 * 16;
    f32x4 bq = *(LAS f32x4*)ba;
    const u32 svo = tid * 16, spo = WS_P + bid * 27648;
    if (w < 3) st16c<0>(u32x4{SENT, SENT, SENT, SENT}, sWS, svo, spo + 24576);
#pragma unroll 1
    for (int left = NC - NQ - 2;; --left) {
      u32x4 un[2];
      float xa, xb, ea, eb;
      pins(left);
      sfor<0, 36>([&](auto KK) __attribute__((always_inline)) {
        constexpr int kk = decltype(KK)::v;
        if constexpr (kk == 33) { if (RARE(left == 1)) wait_vm<0>(); }
        kstep(KK);
        if constexpr (kk >= 2 && kk < 34) {
          constexpr int t = kk - 2, e = 2 * (t >> 2);
          if constexpr ((t & 3) == 0) { xa = __builtin_fmaf(prev[e], rs, bq[e & 3]); ea = gelu_a(xa); pin(ea); }
          if constexpr ((t & 3) == 1) { xb = __builtin_fmaf(prev[e + 1], rs, bq[(e & 3) + 1]); eb = gelu_a(xb); pin(eb); }
          if constexpr ((t & 7) == 5) { if constexpr (t == 29) ba += 256; bq = *(LAS f32x4*)(ba + (((t >> 3) + 1) & 3) * 32); }
          if constexpr ((t & 3) == 2) { ea = gelu_b(xa, ea); pin(ea); }
          if constexpr ((t & 3) == 3) { u32 v = pk2(ea, gelu_b(xb, eb)); pin(v); un[t >> 4][(t >> 2) & 3] = v; }
        }
        if constexpr (kk == 0) {
          if (RARE(left == 0)) { int b_ = bid; pins(b_); if (tid == 0) __hip_atomic_store((GAS u64*)(ws + WS_FLAG + (256 + b_) * 128), tag, RLX_AGENT); }
          if (RARE(left == 1)) {
            int m_ = m, w_ = w, t_ = tid; pins(m_); pins(w_); pin(t_);
            const int l_ = (t_ >> 1) & 7, pm = l_ < 3 ? m_ : (l_ < 5 ? m_ - 1 : m_ - 2);
            fetch_flags(sWS, WS_FLAG + bid_of(pm < 0 ? m_ : pm, kq, l_ < 3 ? l_ : 1 + (l_ & 1)) * 128 + (t_ & 1) * 4, LB + L_FLG + w_ * 64);
          }
        }
        if constexpr (kk == 12) {
          if (RARE(left == 0)) {
            int w_ = w, t_ = tid; pins(w_); pin(t_);
            const u32x4 sB = make_srd(arg(5) + 1152 * kq * 1152, 1152 * 1152);
            const u64 m3 = __builtin_amdgcn_ballot_w64(t_ < 128);
            const u32 o0 = (isq ? 4 * s : 14) * 73728, o1 = (s == 2 ? 14 : 4 * ((s + 1) & 3)) * 73728, dd = LB + (isq ? 2 : 3) * SLOT_B + w_ * 1024;
            sfor<0, 3>([&](auto I_) __attribute__((always_inline)) {
              constexpr int i = decltype(I_)::v;
              const u32 vv = w2_piece(t_ + 512 * i, kq, s);
              if constexpr (i < 2) { dma16<i * 8192, 0>(sB, vv, o0, dd); dma16<SLOT_B + i * 8192, 0>(sB, vv, o1, dd); }
              else { dma16m<16384, 0>(sB, vv, o0, dd, m3); dma16m<SLOT_B + 16384, 0>(sB, vv, o1, dd, m3); }
            });
          }
        }
        if constexpr (kk == 10) { u32x4 sv = {SENT, SENT, SENT, SENT}; pin(sv); st16c<0>(sv, sWS, svo, left < 3 ? spo + left * 8192 : OOB); }
        if constexpr (kk == 22) st16c<0>(un[0], sG, gvo, hso);
        if constexpr (kk == 34) {
          st16c<512>(un[1], sG, gvo, hso);
          hso += 16384;
          so += 128 - 576 * 9216;
          if (RARE(left == 1)) {
            int m_ = m, w_ = w, t_ = tid; pins(m_); pins(w_); pin(t_);
            const u32x2 fl = lds_r8(LB + L_FLG + w_ * 64 + (t_ & 7) * 8);
            const int l_ = t_ & 7, pm = l_ < 3 ? m_ : (l_ < 5 ? m_ - 1 : m_ - 2);
            if (!all_lanes(fl[0] == (u32)tag && fl[1] == (u32)(tag >> 32))) poll_words(ws, WS_FLAG + bid_of(pm < 0 ? m_ : pm, kq, l_ < 3 ? l_ : 1 + (l_ & 1)) * 128, true, tag);
            sA = make_srd(ws + WS_QKV + kq * 16 * QKV_SZ, w_ < 6 ? 16 * QKV_SZ : 0);
            voA = (t_ & 63) * 16; i8 = QKV_SZ; i176 = 1024 - 2 * QKV_SZ;
            so = (m_ - 2) * QKV_SZ + (w_ < 3 ? 36 + 3 * w_ : 63 + 3 * w_) * 1024 + 9216 * s - i176;
            D0 = w_ < 6 ? LB + L_KV + 3 * w_ * PP : L_TOTAL; D1 = D0 + PP; D2 = D1 + PP;
            if (w_ < 6) *(LAS u32x2*)(LB + L_KV + 18 * PP + t_ * 8) = u32x2{t_ < 256 - 128 * m_ ? 0xff7f3f80u : 0x00003f80u, 0x3f803f80u};
          }
        }
        fence_sched();
        if constexpr (kk == 11) { wait_vm<5>(); bar(); }
        if constexpr (kk == 23) {
          if (left) { wait_vm<5>(); bar(); }
          else { int w_ = w, b_ = bid, t_ = tid; pins(w_); pins(b_); pin(t_); fetch_flags(sWS, WS_FLAG + (256 + (b_ & ~24)) * 128 + ((t_ >> 1) & 3) * 1024 + (t_ & 1) * 4, LB + L_FLG + w_ * 64); }
        }
      });
      if (left == 0) break;
      wait_vm<5>(); bar();
    }
  }
  bar();
  const auto rWS = __builtin_amdgcn_make_buffer_rsrc(ws, 0, WS_END, 0x00020000);
  u32 ltab = mul24(*(LAS u32*)(LB + L_TAB + (l & 31) * 4), c ? 73728 : 16384);
  pin(ltab);
  auto line = [&](int n) -> u32 { return __builtin_amdgcn_readlane(ltab, n); };
  u32x4 un[2];
  auto last_gelu = [&](auto&& between) __attribute__((always_inline)) {
    asm volatile("s_nop 7\n\ts_nop 3" ::: "memory");
    sfor<0, 4>([&](auto G_) __attribute__((always_inline)) {
      constexpr int gq = decltype(G_)::v;
      between(G_);
      const f32x4 b = *(LAS f32x4*)(ba + gq * 32);
      float y[4];
#pragma unroll
      for (int e = 0; e < 4; ++e) { const float xx = __builtin_fmaf(prev[4 * gq + e], rs, b[e]); y[e] = gelu_b(xx, gelu_a(xx)); }
      u32 u0 = pk2(y[0], y[1]), u1 = pk2(y[2], y[3]); pin(u0); pin(u1);
      un[gq >> 1][2 * (gq & 1)] = u0; un[gq >> 1][2 * (gq & 1) + 1] = u1;
      fence_sched();
    });
    wait_vm<4>(); wait_lgkm0(); bar();
    st16c<0>(un[0], sG, gvo, hso); st16c<512>(un[1], sG, gvo, hso);
  };
  f32x4 v0, v1, v8;
  if (c == 0) {
    const auto rG = __builtin_amdgcn_make_buffer_rsrc(ws + WS_G + (kq * 16 + m) * G_SZ, 0, G_SZ, 0x00020000);
    const u32 gl0 = 2 * r * 1024 + l * 16, gl1 = gl0 + 8192;
    u32x4 gf[3][4];
#define ISSUE_G(sl_, i, off) gf[sl_][i] = __builtin_amdgcn_raw_buffer_load_b128(rG, ((i) & 2 ? gl1 : gl0) + ((i) & 1) * 1024, off, 16)
    u32 wb = LB + (16 * (g4 >> 1) + 4 * (g4 & 1) + q4) * 288 + 8 * p4;
    u32 wo1 = LB + L_KV + (20 * (g4 >> 1) + 4 * (g4 & 1) + q4) * 288 + 8 * p4, wo2 = LB + L_KV + (36 * g4 + 16 + q4) * 288 + 8 * p4;
    pin(wb); pin(wo1); pin(wo2);
    f32x4 ac[2][9];
#pragma unroll
    for (int d = 0; d < 9; ++d) { ac[0][d] = f32x4{}; ac[1][d] = f32x4{}; pin(ac[0][d]); pin(ac[1][d]); }
    { const u32 go = line(0); ISSUE_G(0, 0, go); ISSUE_G(0, 1, go); ISSUE_G(0, 2, go); ISSUE_G(0, 3, go); }
    wait_vm<8>();
    { const u32x2 fl = lds_r8(LB + L_FLG + w * 64 + (l & 3) * 8);
      if (RARE(!all_lanes(fl[0] == (u32)tag && fl[1] == (u32)(tag >> 32)))) poll_words(ws, WS_FLAG + (256 + (bid & ~24)) * 128 + (l & 3) * 1024, l < 4, tag); }
    last_gelu([&](auto G_) __attribute__((always_inline)) {
      constexpr int g = decltype(G_)::v + 1;
      if constexpr (g < 2) { const u32 go = line(g); ISSUE_G(g, 0, go); ISSUE_G(g, 1, go); ISSUE_G(g, 2, go); ISSUE_G(g, 3, go); }
    });
    u32 rbo = (isq ? 2 : 3) * SLOT_B, cur = wb + rbo;
    bf16x8 af[9];
#pragma unroll
    for (int d = 0; d < 8; ++d) af[d] = tr_frag(cur + d * 32, cur + d * 32 + 2304);
    auto step = [&](auto Q_, auto D_, const u32x4& b0, const u32x4& b1, u32 pa, auto PRE) __attribute__((always_inline)) {
      constexpr int a = decltype(Q_)::v % 9, dn = (a + 8) % 9, d = decltype(D_)::v;
      s16x4 lo, hi;
      mfma16_acc(ac[0][d], af[a], b0); fence_sched();
      if constexpr (decltype(PRE)::v) lo = __builtin_amdgcn_ds_read_tr16_b64_v4i16((LAS s16x4*)pa);
      fence_sched();
      mfma16_acc(ac[1][d], af[a], b1); fence_sched();
      if constexpr (decltype(PRE)::v) { hi = __builtin_amdgcn_ds_read_tr16_b64_v4i16((LAS s16x4*)(pa + 2304)); af[dn] = __builtin_bit_cast(bf16x8, __builtin_shufflevector(lo, hi, 0, 1, 2, 3, 4, 5, 6, 7)); }
      fence_sched();
    };
#pragma unroll 1
    for (int n0 = 0; n0 < 18; n0 += 3) {
      sfor<0, 3>([&](auto B_) __attribute__((always_inline)) {
        constexpr int b = decltype(B_)::v;
        u32 nxt, go;
        sfor<0, 18>([&](auto Q_) __attribute__((always_inline)) {
          constexpr int q = decltype(Q_)::v, qn = q + 8;
          if constexpr (q == 9) { rbo = rbo == 4 * SLOT_B ? 0 : rbo + SLOT_B; nxt = (b == 2 && n0 == 15) ? wo1 : wb + rbo; pin(nxt); }
          step(Q_, IC<q % 9>{}, gf[b][2 * (q / 9)], gf[b][2 * (q / 9) + 1], qn < 18 ? cur + (qn / 9) * 9216 + (qn % 9) * 32 : nxt + (qn - 18) * 32, IC<1>{});
          if constexpr (q == 0) go = line(n0 + b + 2);
          if constexpr (q == 1 || q == 5 || q == 9 || q == 13) ISSUE_G((b + 2) % 3, (q - 1) / 4, go);
          fence_sched();
        });
        cur = nxt;
        if constexpr (b == 1) { if (RARE(n0 == 0)) wait_vm<0>(); }
        bar();
      });
    }
    const u32 pbase = WS_P + r * 1024;
    sfor<0, 45>([&](auto Q_) __attribute__((always_inline)) {
      constexpr int q = decltype(Q_)::v, ks = o_ks(q), qn = q + 8, kn = o_ks(qn);
      step(Q_, IC<o_d(q)>{}, gf[ks / 2][2 * (ks & 1)], gf[ks / 2][2 * (ks & 1) + 1], (kn < 4 ? wo1 + kn * 36 * 288 : wo2) + o_d(qn) * 32, IC<(qn < 45)>{});
      if constexpr (q == 1 || q == 4) ISSUE_G(2, (q - 1) / 3, 20 * 16384);
      if constexpr (q == 33 || q == 35 || q == 37) {
        constexpr int k = (q - 31) / 2;
        asm volatile("s_nop 3" ::: "memory"); fence_sched();
#pragma unroll
        for (int e = 0; e < 2; ++e)
          st16c<0>(u32x4{umin(pk2(ac[e][2 * k][0], ac[e][2 * k][1]), SENT - 1), umin(pk2(ac[e][2 * k][2], ac[e][2 * k][3]), SENT - 1), umin(pk2(ac[e][2 * k + 1][0], ac[e][2 * k + 1][1]), SENT - 1), umin(pk2(ac[e][2 * k + 1][2], ac[e][2 * k + 1][3]), SENT - 1)},
                   sWS, l * 16 + e * 4096, pbase + (bid ^ k) * 27648 + (k - 1) * 9216);
      }
      if constexpr (q == 38) {
        asm volatile("s_nop 3" ::: "memory"); fence_sched();
#pragma unroll
        for (int e = 0; e < 2; ++e)
          st8c<0>(u32x2{umin(pk2(ac[e][8][0], ac[e][8][1]), SENT - 1), umin(pk2(ac[e][8][2], ac[e][8][3]), SENT - 1)}, sWS, l * 8 + e * 512, r == kq ? OOB : WS_P + (bid ^ kq ^ r) * 27648 + ((kq ^ r) - 1) * 9216 + 8192);
        *(LAS f32x4*)(LB + r * 3072 + 2048 + l * 16) = ac[1][8];
      }
      fence_sched();
    });
    asm volatile("s_nop 7" ::: "memory"); fence_sched();
    *(LAS f32x4*)(LB + r * 3072 + l * 16) = ac[1][0]; *(LAS f32x4*)(LB + r * 3072 + 1024 + l * 16) = ac[1][1];
    v0 = ac[0][0]; v1 = ac[0][1]; v8 = ac[0][8];
    wait_lgkm0(); bar();
  } else {
    __builtin_amdgcn_s_setprio(1);
    u32 vb[5];
#pragma unroll
    for (int i = 0; i < 5; ++i) vb[i] = w2_piece(64 * (r + 4 * i) + l, kq, s);
    const u64 m5 = __builtin_amdgcn_ballot_w64(tid < 384);
    const u32x4 sB = make_srd(arg(5) + 1152 * kq * 1152, 1152 * 1152);
    u32 wso, wd = LB + r * 1024 + (isq ? 4 : 0) * SLOT_B;
    int wn = 2;
    auto issue_w = [&](auto I_) __attribute__((always_inline)) {
      constexpr int i = decltype(I_)::v;
      if constexpr (i == 0) wso = line(wn);
      if constexpr (i < 4) dma16<i * 4096, 0>(sB, vb[i], wso, wd);
      else { dma16m<16384, 0>(sB, vb[4], wso, wd, m5); ++wn; wd = wd >= 4 * SLOT_B ? wd - 4 * SLOT_B : wd + SLOT_B; }
    };
#define ISSUE_W(i) issue_w(IC<(i)>{})
    u32x2 q5[5];
    {
      const u32 qo = WS_QKV + (kq * 16 + m) * QKV_SZ + 9216 * s, qv = (32 * r + (l & 31)) * 8;
      q5[0] = ld8c<0>(sWS, qv + 2 * h * 1024, qo); q5[1] = ld8c<1024>(sWS, qv + 2 * h * 1024, qo);
      q5[2] = ld8c<0>(sWS, qv + 2 * h * 1024, qo + 5120); q5[3] = ld8c<1024>(sWS, qv + 2 * h * 1024, qo + 5120);
      q5[4] = ld8c<0>(sWS, h ? OOB : qv, qo + 4096);
    }
    last_gelu([&](auto G_) __attribute__((always_inline)) {
      ISSUE_W(decltype(G_)::v); if constexpr (decltype(G_)::v == 3) ISSUE_W(4);
    });
#pragma unroll
    for (int i = 0; i < 5; ++i) pin(q5[i]);
    const u32x4 qf[2] = {cat(q5[0], q5[1]), cat(q5[2], q5[3])};
    const u32x2 qq = q5[4];
    const u32 fvo = WS_FLAG + (768 + (bid & ~24)) * 128 + ((l >> 1) & 3) * 1024 + (l & 1) * 4;
    u32 ka = LB + L_KV + (32 * r + (l & 31)) * 8, kb = ka + 4 * PP;
    ka += 2 * h * PP;
    u32 va = LB + L_KV + (32 * r + 4 * h + q4) * 8 + (blk ? (p4 ? 18 : 13) : 9 + p4) * PP;
    u32x4 qm_ = qf[0], qt = {qq[0], 0u, h ? 0u : 0x3f800000u, 0u};
    u32 oso = (36 + s) * 8192, tso = OOB, tl0 = 0;
#define K_MAIN(i) as_bf(cat(lds_r8(ka + (i) * 256), lds_r8(ka + PP + (i) * 256)))
#define K_TAIL(i) as_bf(cat(lds_r8(kb + (i) * 256), lds_r8(kb + 14 * PP + (i) * 256)))
#pragma unroll 1
    for (int hd = 0; hd < 2; ++hd) {
      pin(ka); pin(kb); pin(va);
      bf16x8 km[3], kt[3], k8;
      f32x16 st[9];
      km[0] = K_MAIN(0); kt[0] = K_TAIL(0); km[1] = K_MAIN(8); kt[1] = K_TAIL(8); k8 = kt[1];
      float mx0, mx1;
      sfor<0, 11>([&](auto I_) __attribute__((always_inline)) {
        constexpr int ii = decltype(I_)::v;
        if constexpr (ii + 2 < 9) { km[(ii + 2) % 3] = K_MAIN(ii + 1); kt[(ii + 2) % 3] = K_TAIL(ii + 1); }
        if constexpr (ii < 9) {
          st[ii] = MFMA32(km[ii % 3], as_bf(qm_), f32x16{});
          st[ii] = MFMA32(kt[ii % 3], as_bf(qt), st[ii]);
          pin(st[ii]);
        }
        if constexpr (ii < 10) ISSUE_W(ii % 5);
        if constexpr (ii == 3) {
#pragma unroll
          for (int e = 0; e < 16; ++e) st[1][e] = __builtin_amdgcn_inverse_ballot_w64(diag_mask(e)) ? st[1][e] : st[0][e];
          mx0 = st[1][0]; mx1 = st[1][1];
        }
        if constexpr (ii >= 3) {
          constexpr int i = ii - 2;
#pragma unroll
          for (int e = (ii == 3 ? 2 : 0); e < 16; e += 2) { if (e & 2) mx0 = max3(mx0, st[i][e], st[i][e + 1]); else mx1 = max3(mx1, st[i][e], st[i][e + 1]); }
          pin(mx0); pin(mx1);
        }
        fence_sched();
        if constexpr (ii == 4) { wait_vm<4>(); bar(); if (hd == 0) wait_vm<0>(); }
      });
      float mx;
      { auto sw = __builtin_amdgcn_permlane32_swap(f2u(mx0), f2u(mx1), false, false); mx = max3(u2f(sw[0]), u2f(sw[1]), u2f(sw[0]));
        auto s2 = __builtin_amdgcn_permlane32_swap(f2u(mx), f2u(mx), false, false); mx = max3(u2f(s2[0]), u2f(s2[1]), u2f(s2[0])); }
      u32x4 qm = {};
      { const float n0 = -mx, a0 = bhi(f2u(n0)), n1 = n0 - a0, a1 = bhi(f2u(n1)), n2 = n1 - a1;
        qm[2] = h ? 0u : f2u(a0) >> 16; qm[3] = h ? 0u : ((f2u(a1) >> 16) | (f2u(n2) & 0xffff0000u)); }
      st[1] = MFMA32(k8, as_bf(qm), st[1]); st[2] = MFMA32(k8, as_bf(qm), st[2]); pin(st[1]); pin(st[2]);
      bf16x8 vf[2], v0f[2];
      vf[0] = tr_frag(va + 8 * 256, va + 8 * 256 + 64);
      v0f[0] = tr_frag(va, va + 64); v0f[1] = tr_frag(va + 128, va + 192);
      const u32x4 mk0 = lds_r16(LB + L_MSK + l * 16), mk1 = lds_r16(LB + L_MSK + 1024 + l * 16);
      wait_vm<4>(); bar();
      if (tid == 256) __hip_atomic_store((GAS u64*)(ws + WS_FLAG + (768 + bid) * 128), tag, RLX_AGENT);
      f32x16 o;
      sfor<0, 16>([&](auto J_) __attribute__((always_inline)) {
        constexpr int j = decltype(J_)::v, i = 1 + (j >> 1), u = j & 1, jn = j + 1, tn = (jn >> 1) == 0 ? 8 : (jn >> 1);
        if constexpr (jn < 16) vf[jn & 1] = tr_frag(va + tn * 256 + (jn & 1) * 128, va + tn * 256 + (jn & 1) * 128 + 64);
        if constexpr (u == 0 && i + 2 < 9) { st[i + 2] = MFMA32(k8, as_bf(qm), st[i + 2]); pin(st[i + 2]); }
        ISSUE_W(j % 4); if constexpr (j % 4 == 3) ISSUE_W(4);
        u32x4 pf;
#pragma unroll
        for (int e = 0; e < 4; ++e) pf[e] = pk2(__builtin_amdgcn_exp2f(st[i][8 * u + 2 * e]), __builtin_amdgcn_exp2f(st[i][8 * u + 2 * e + 1]));
        if constexpr (i == 1) {
          const u32x4 mk = u ? mk1 : mk0;
          if constexpr (j == 0) o = MFMA32(v0f[u], as_bf(pf & ~mk), f32x16{}); else o = MFMA32(v0f[u], as_bf(pf & ~mk), o);
          pf &= mk;
        }
        o = MFMA32(vf[u], as_bf(pf), o); pin(o);
        fence_sched();
        if constexpr (j == 11) {
          wait_vm<4>();
          const u32x2 fl = lds_r8(LB + L_FLG + r * 64 + (l & 3) * 8);
          if (RARE(!all_lanes(fl[0] == (u32)tag && fl[1] == (u32)(tag >> 32)))) poll_words(ws, fvo & ~4u, l < 8, tag);
        }
        if constexpr (j % 4 == 3 && j < 15) { wait_vm<4>(); bar(); }
        if constexpr (j == 7) fetch_flags(sWS, fvo, LB + L_FLG + r * 64);
      });
      const float inv = __builtin_amdgcn_rcpf(h ? o[8] : o[12]);
      st16c<0>(u32x4{pk2(o[0] * inv, o[1] * inv), pk2(o[2] * inv, o[3] * inv), pk2(o[4] * inv, o[5] * inv), pk2(o[6] * inv, o[7] * inv)}, sG, gvo, oso);
      st16c<0>(u32x4{tl0, pk2(o[10] * inv, o[11] * inv), 0u, 0u}, sG, h ? OOB : gvo, tso);
      tl0 = pk2(o[8] * inv, o[9] * inv);
      oso += 512; tso = 40 * 8192 + s * 256;
      touch(make_srd(arg(2) + 144 * kq * 1152, 144 * 1152), l < 6 ? ((((bid >> 3) * 4 + r) * 2 + hd) * 6 + l) * 128 : OOB, LB + L_DUMP);
      ka += 5 * PP; va += blk ? 0 : 5 * PP; qm_ = qf[1]; qt[0] = 0u; qt[1] = qq[1];
      wait_vm<7>(); bar();
    }
    const u32x4 sWO = make_srd(arg(2) + 144 * kq * 1152, 144 * 1152), sX = make_srd(arg(0), 2048 * 1152);
    const u32 dw = LB + L_KV + r * 1024;
    auto issue_o = [&](auto I_) __attribute__((always_inline)) {
      constexpr int i = decltype(I_)::v, sl = i / 5, k = i % 5;
      if constexpr (k < 4 && i != 11) dma16<sl * SLOT_B + k * 4096, sl * 73728>(sWO, vb[k], 0u, dw); else dma16m<sl * SLOT_B + k * 4096, sl * 73728>(sWO, vb[k], 0u, dw, m5);
      __builtin_amdgcn_s_sleep(1);
    };
    wait_vm<0>();
    sfor<0, 10>([&](auto I_) __attribute__((always_inline)) { ISSUE_W(decltype(I_)::v % 5); });
    bar();
    if (tid == 256) __hip_atomic_store((GAS u64*)(ws + WS_FLAG + (512 + bid) * 128), tag, RLX_AGENT);
    sfor<0, 5>([&](auto I_) __attribute__((always_inline)) { ISSUE_W(decltype(I_)::v); __builtin_amdgcn_s_sleep(1); });
    wait_vm<8>(); bar();
    sfor<0, 7>(issue_o);
    wait_vm<10>(); bar();
    if (r == 0) poll_words(ws, WS_FLAG + ((l & 4) ? (768 + (bid & ~3) + (l & 3)) * 128 : (512 + (bid & ~24)) * 128 + (l & 3) * 1024), l < 8, tag);
    sfor<7, 12>(issue_o);
    wait_vm<9>(); bar();
    dma16<L_RES, 0>(sX, (l >> 2) * 1152 + p4 * 16 + 288 * s + 64 * kq, xso - 16 * 1152, LB + 2 * r * 1024);
    dma16<L_RES + 1024, 0>(sX, (l >> 2) * 1152 + p4 * 16 + 288 * s + 64 * kq, xso, LB + 2 * r * 1024);
    if (r == 0) dma16<L_RES + 8192, 0>(sX, (l >> 1) * 1152 + (l & 1) * 16 + 288 * s + 256, (128 * m + 32 * kq) * 1152, LB);
    if (r == 1) dma16<L_RES + 9216, 0>(make_srd(arg(6), 1152), l * 16 + 288 * s, 0, LB);
    wait_vm<2>(); bar();
    bar();
    if (bid == 24 && r == 1) {
      poll_words(ws, WS_FLAG + (512 + (l & 7) + (l >> 3) * 32) * 128, true, tag);
      if (l == 0) __hip_atomic_store((GAS u64*)(ws + WS_EPOCH), tag, RLX_AGENT);
    }
    wait_vm<0>(); bar();
    v0 = *(LAS f32x4*)(LB + r * 3072 + l * 16); v1 = *(LAS f32x4*)(LB + r * 3072 + 1024 + l * 16); v8 = *(LAS f32x4*)(LB + r * 3072 + 2048 + l * 16);
  }
  {
    const bool own8 = r == kq;
    const u32 ra = LB + L_RES + (2 * r + c) * 1024 + t16 * 64 + g4 * 8, r8 = LB + L_RES + 8192 + (16 * c + t16) * 32 + g4 * 8, rb = LB + L_RES + 9216 + g4 * 8;
    {
      const u32x2 x0 = lds_r8(ra), x1 = lds_r8(ra + 32), x8 = lds_r8(r8), b0 = lds_r8(rb + 64 * kq), b1_ = lds_r8(rb + 64 * kq + 32), b8 = lds_r8(rb + 256);
      v0 += f32x4{blo(x0[0]), bhi(x0[0]), blo(x0[1]), bhi(x0[1])} + f32x4{blo(b0[0]), bhi(b0[0]), blo(b0[1]), bhi(b0[1])};
      v1 += f32x4{blo(x1[0]), bhi(x1[0]), blo(x1[1]), bhi(x1[1])} + f32x4{blo(b1_[0]), bhi(b1_[0]), blo(b1_[1]), bhi(b1_[1])};
      v8 += f32x4{blo(x8[0]), bhi(x8[0]), blo(x8[1]), bhi(x8[1])} + f32x4{blo(b8[0]), bhi(b8[0]), blo(b8[1]), bhi(b8[1])};
      pin(v0); pin(v1); pin(v8);
    }
    const u32x4 sOUT = make_srd(arg(7), 2048 * 1152);
    u32 ov = (l >> 2) * 1152 + p4 * 16 + 288 * s + 64 * kq, ov8 = (own8 && l < 32) ? (l >> 1) * 1152 + (l & 1) * 16 + 288 * s + 256 : OOB;
    pin(ov); pin(ov8);
    const u32 po = WS_P + bid * 27648;
    u32x4 pa[3]; u32x2 pb[3];
#pragma unroll 1
    for (u32 spins = 0; spins < SPINS; ++spins) {
      asm volatile("" ::: "memory");
#pragma unroll
      for (int sg = 0; sg < 3; ++sg) {
        pa[sg] = __builtin_amdgcn_raw_buffer_load_b128(rWS, l * 16, po + sg * 9216 + w * 1024, 16);
        pb[sg] = __builtin_amdgcn_raw_buffer_load_b64(rWS, l * 8, own8 ? po + sg * 9216 + 8192 + c * 512 : OOB, 16);
      }
      u32 mxu = 0;
#pragma unroll
      for (int sg = 0; sg < 3; ++sg) mxu = umax(umax(umax(mxu, umax(pa[sg][0], pa[sg][1])), umax(pa[sg][2], pa[sg][3])), umax(pb[sg][0], pb[sg][1]));
      if (all_lanes(mxu != SENT)) break;
      __builtin_amdgcn_s_sleep(1);
    }
#pragma unroll
    for (int sg = 0; sg < 3; ++sg) {
      v0 += f32x4{blo(pa[sg][0]), bhi(pa[sg][0]), blo(pa[sg][1]), bhi(pa[sg][1])};
      v1 += f32x4{blo(pa[sg][2]), bhi(pa[sg][2]), blo(pa[sg][3]), bhi(pa[sg][3])};
    }
    *(LAS u32x2*)ra = u32x2{pk2(v0[0], v0[1]), pk2(v0[2], v0[3])};
    *(LAS u32x2*)(ra + 32) = u32x2{pk2(v1[0], v1[1]), pk2(v1[2], v1[3])};
    const u32x4 t = lds_r16(LB + L_RES + (2 * r + c) * 1024 + l * 16);
    asm volatile("s_nop 4\n\tbuffer_store_dwordx4 %0, %1, %2, %3 offen sc1\n\ts_nop 1" :: "v"(t), "v"(ov), "s"(sOUT), "s"(xso) : "memory");
    if (own8) {
#pragma unroll
      for (int sg = 0; sg < 3; ++sg) v8 += f32x4{blo(pb[sg][0]), bhi(pb[sg][0]), blo(pb[sg][1]), bhi(pb[sg][1])};
      *(LAS u32x2*)r8 = u32x2{pk2(v8[0], v8[1]), pk2(v8[2], v8[3])};
      const u32x4 t8 = lds_r16(LB + L_RES + 8192 + c * 512 + (l & 31) * 16);
      asm volatile("s_nop 4\n\tbuffer_store_dwordx4 %0, %1, %2, %3 offen sc1\n\ts_nop 1" :: "v"(t8), "v"(ov8), "s"(sOUT), "s"(xso) : "memory");
    }
  }
}
#if defined(__HIP_DEVICE_COMPILE__)
#pragma clang attribute pop
#endif

extern "C" void kernel_launch(void* const* d_in, const int* in_sizes, int n_in, void* d_out, int out_size, void* d_ws, size_t ws_size, hipStream_t stream) {
  fused_block<<<256, 512, 0, stream>>>((const u32*)d_in[0], (const u32*)d_in[1], (const u32*)d_in[2], (const u32*)d_in[4], (const u32*)d_in[5],
                                       (const u32*)d_in[6], (const u32*)d_in[7], (u32*)d_out, (char*)d_ws);
}
```
